```python
import torch

def _softmax(x, axis=-1):
    return torch.softmax(x, dim=axis)

def _gelu(x):
    return torch.nn.functional.gelu(x, approximate='tanh')

def _silu(x):
    return torch.nn.functional.silu(x)

def _sigmoid(x):
    return torch.sigmoid(x)

def _layernorm(x, axis=-1, eps=1e-05):
    m = x.mean(dim=axis, keepdim=True)
    v = x.var(dim=axis, keepdim=True, unbiased=False)
    return (x - m) / torch.sqrt(v + eps)

def _scatter_add(tgt, idx, src, axis=0):
    return tgt.clone().index_add_(axis, idx, src)

def _segment_sum(src, seg, n):
    out = torch.zeros((n,) + src.shape[1:], dtype=src.dtype)
    return out.index_add_(0, seg, src)

def _pad(x, pw):
    flat = tuple((v for pair in reversed(pw) for v in pair))
    return torch.nn.functional.pad(x, flat)
kernel_name = 'adaLN_cond__ssm_conv_s4__glu_gated_swiglu__prompt'

def setup_inputs():

    _w = lambda a: (lambda t: t.to(torch.bfloat16) if t.is_floating_point() else t)(torch.as_tensor(a))

    def _A_setup():
        B, L, D, N = (1, 2048, 576, 128)
        A_re = -(lambda _x: torch.exp(torch.as_tensor(_x)))((lambda *s: torch.randn(*s, dtype=torch.bfloat16) * 3.2)(D, N))
        A_im = torch.pi * (lambda *s: torch.randn(*s, dtype=torch.bfloat16) * 0.1)(D, N)
        B_re = (lambda *s: torch.randn(*s, dtype=torch.bfloat16) * 0.1)(D, N)
        B_im = (lambda *s: torch.randn(*s, dtype=torch.bfloat16) * 0.1)(D, N)
        C_re = (lambda *s: torch.randn(*s, dtype=torch.bfloat16) * 0.1)(D, N)
        C_im = (lambda *s: torch.randn(*s, dtype=torch.bfloat16) * 0.1)(D, N)
        log_dt = (lambda *s: torch.randn(*s, dtype=torch.bfloat16) * 0.1)(D) * 0.5 - 4.0
        Dskip = (lambda *s: torch.randn(*s, dtype=torch.bfloat16) * 0.1)(D)
        return {'u': _w((lambda *s: torch.randn(*s, dtype=torch.bfloat16) * 0.05)(B, L, D)), 'A_re': _w(A_re), 'A_im': _w(A_im), 'B_re': _w(B_re), 'B_im': _w(B_im), 'C_re': _w(C_re), 'C_im': _w(C_im), 'log_dt': _w(log_dt), 'Dskip': _w(Dskip)}
    _A_in = _A_setup()

    def _B_setup():
        B, S, D, F = (1, 2048, 576, 4608)
        return {'x': _w((lambda *s: torch.randn(*s, dtype=torch.bfloat16) * 0.05)(B, S, D)), 'w1': _w((lambda *s: torch.randn(*s, dtype=torch.bfloat16) * 1.6)(D, F) * (2.0 / D) ** 0.5), 'w3': _w((lambda *s: torch.randn(*s, dtype=torch.bfloat16) * 1.6)(D, F) * (2.0 / D) ** 0.5), 'w2': _w((lambda *s: torch.randn(*s, dtype=torch.bfloat16) * 1.6)(F, D) * (2.0 / F) ** 0.5)}
    _B_in = _B_setup()
    return {'A_u': _A_in['u'], 'A_A_re': _A_in['A_re'], 'A_A_im': _A_in['A_im'], 'A_B_re': _A_in['B_re'], 'A_B_im': _A_in['B_im'], 'A_C_re': _A_in['C_re'], 'A_C_im': _A_in['C_im'], 'A_log_dt': _A_in['log_dt'], 'A_Dskip': _A_in['Dskip'], 'B_x': _B_in['x'], 'B_w1': _B_in['w1'], 'B_w3': _B_in['w3'], 'B_w2': _B_in['w2'], 'c': _w((lambda *s: torch.randn(*s, dtype=torch.bfloat16) * 0.4)(1, 576) * 8.0), 'w_mod': _w((lambda *s: torch.randn(*s, dtype=torch.bfloat16) * 0.4)(576, 6 * 576) * 576 ** (-0.5) * 8.0)}

def reference(A_u, A_A_re, A_A_im, A_B_re, A_B_im, A_C_re, A_C_im, A_log_dt, A_Dskip, B_x, B_w1, B_w3, B_w2, c, w_mod):

    def _A_ref(u, A_re, A_im, B_re, B_im, C_re, C_im, log_dt, Dskip):
        Bsz, L, D = u.shape
        dt = torch.exp(log_dt)[:, None]
        A = A_re + 1j * A_im
        Bc = B_re + 1j * B_im
        Cc = C_re + 1j * C_im
        logdA = dt * A
        dB = dt * Bc
        l = (lambda x, dt: x.to(dt))(torch.arange(L), A_re.dtype)
        P = torch.exp(logdA[:, :, None] * l[None, None, :])
        K = torch.einsum('dn,dnl,dn->dl', Cc, P, dB).real
        L2 = 2 * L
        Kp = _pad(K, ((0, 0), (0, L)))
        up = (lambda x, *d: x.permute(*d))(u, 0, 2, 1)
        up = _pad(up, ((0, 0), (0, 0), (0, L)))
        Kf = (lambda x, axes=(-2, -1): torch.fft.rfft2(x.float(), dim=axes))(Kp[:, None, :], axes=(-2, -1))
        Uf = (lambda x, axes=(-2, -1): torch.fft.rfft2(x.float(), dim=axes))(up[:, :, None, :], axes=(-2, -1))
        Yf = Uf * Kf[None, :, :, :]
        y = (lambda x, s, axes=(-2, -1): torch.fft.irfft2(x, s=s, dim=axes).to(torch.bfloat16))(Yf, (1, L2), axes=(-2, -1))[:, :, 0, :L]
        y = (lambda x, *d: x.permute(*d))(y, 0, 2, 1) + u * Dskip[None, None, :]
        return y

    def _B_ref(x, w1, w3, w2):
        gate = torch.einsum('bsd,df->bsf', x, w1)
        up = torch.einsum('bsd,df->bsf', x, w3)
        h = _silu(gate) * up
        out = torch.einsum('bsf,fd->bsd', h, w2)
        return out
    x = A_u
    mod = (_silu(c) @ w_mod).reshape(c.shape[0], 6, 1, -1)
    sa, ca, ga, sb, cb, gb = (mod[:, 0].clone(), mod[:, 1].clone(), mod[:, 2].clone(), mod[:, 3].clone(), mod[:, 4].clone(), mod[:, 5].clone())
    h = _layernorm(x, axis=-1) * (1 + ca) + sa
    x = x + ga * _A_ref(h, A_A_re, A_A_im, A_B_re, A_B_im, A_C_re, A_C_im, A_log_dt, A_Dskip)
    h = _layernorm(x, axis=-1) * (1 + cb) + sb
    x = x + gb * _B_ref(h, B_w1, B_w3, B_w2)
    return x
```

```cpp
#include <hip/hip_runtime.h>
#include <hip/hip_bf16.h>

typedef unsigned short u16;
typedef unsigned int u32;
typedef __attribute__((ext_vector_type(2))) float f32x2;
typedef __attribute__((ext_vector_type(4))) float f32x4;
typedef __attribute__((ext_vector_type(2))) u32 u32x2;
typedef __attribute__((ext_vector_type(4))) u32 u32x4;
typedef __attribute__((ext_vector_type(4))) short s16x4;
typedef __attribute__((ext_vector_type(8))) short s16x8;
typedef __attribute__((ext_vector_type(2))) __bf16 bf16x2;
typedef __attribute__((ext_vector_type(8))) __bf16 bf16x8;
typedef __attribute__((ext_vector_type(2))) _Float16 f16x2;
typedef __attribute__((ext_vector_type(8))) _Float16 f16x8;
#define LAS __attribute__((address_space(3)))
#define GAS __attribute__((address_space(1)))

constexpr int L = 2048, D = 576, NM = 128, F = 4608, MODN = 6 * D;
constexpr int KSPLIT = 3;

constexpr size_t WS_MODP = 0;
constexpr size_t WS_MODF = 65536;
constexpr size_t WS_KSC  = WS_MODF + 16384;
constexpr size_t WS_XHT  = 131072;
constexpr size_t ACT_B   = (size_t)L * D * 2;
constexpr size_t WS_KX   = WS_XHT + ACT_B;
constexpr size_t WS_RT   = WS_KX + ACT_B;
constexpr size_t WS_X1   = WS_RT + ACT_B;
constexpr size_t WS_H2T  = WS_X1 + ACT_B;
constexpr size_t WS_GT   = WS_H2T + ACT_B;

__device__ __forceinline__ float bf2f(u16 v) { return __builtin_bit_cast(float, (u32)v << 16); }
__device__ __forceinline__ float bflo(u32 v) { return __builtin_bit_cast(float, v << 16); }
__device__ __forceinline__ float bfhi(u32 v) { return __builtin_bit_cast(float, v & 0xffff0000u); }
__device__ __forceinline__ u32 pk_bf(float a, float b) { f32x2 v = {a, b}; return __builtin_bit_cast(u32, __builtin_convertvector(v, bf16x2)); }
__device__ __forceinline__ u32 pk_h(float a, float b) { f32x2 v = {a, b}; return __builtin_bit_cast(u32, __builtin_convertvector(v, f16x2)); }
__device__ __forceinline__ float bfr(float a) { return bflo(pk_bf(a, a)); }

template <int CTRL> __device__ __forceinline__ float dpp(float v) {
  return __builtin_bit_cast(float, __builtin_amdgcn_update_dpp(0, __builtin_bit_cast(int, v), CTRL, 0xf, 0xf, true));
}
__device__ __forceinline__ float sum16x(float v) {
  u32 u = __builtin_bit_cast(u32, v);
  auto r = __builtin_amdgcn_permlane16_swap(u, u, false, false);
  return __builtin_bit_cast(float, (u32)r[0]) + __builtin_bit_cast(float, (u32)r[1]);
}
__device__ __forceinline__ float sum32x(float v) {
  u32 u = __builtin_bit_cast(u32, v);
  auto r = __builtin_amdgcn_permlane32_swap(u, u, false, false);
  return __builtin_bit_cast(float, (u32)r[0]) + __builtin_bit_cast(float, (u32)r[1]);
}
__device__ __forceinline__ float wave_sum(float v) {
  v += dpp<0xB1>(v);
  v += dpp<0x4E>(v);
  v += dpp<0x141>(v);
  v += dpp<0x140>(v);
  return sum32x(sum16x(v));
}
__device__ __forceinline__ float sum_hi3(float v) {
  v += dpp<0x128>(v);
  return sum32x(sum16x(v));
}
__device__ __forceinline__ float silu_f(float g) { return g * __builtin_amdgcn_rcpf(1.0f + __builtin_amdgcn_exp2f(-1.4426950408889634f * g)); }

constexpr int K1_MOD = 54 * KSPLIT, K1_LN = 256, K1_KG = D;
constexpr int TROW = 528;

struct cplx { float re, im; };
__device__ __forceinline__ cplx cmul(cplx a, cplx b) { return {a.re * b.re - a.im * b.im, a.re * b.im + a.im * b.re}; }
__device__ __forceinline__ cplx cexpl(float al2, float brev, float l) {
  float m = __builtin_amdgcn_exp2f(al2 * l);
  float ph = __builtin_amdgcn_fractf(brev * l);
  return {m * __builtin_amdgcn_cosf(ph), m * __builtin_amdgcn_sinf(ph)};
}

__global__ __launch_bounds__(256) void k1_kernel(const u16* __restrict__ x, const u16* __restrict__ Are, const u16* __restrict__ Aim,
                                                 const u16* __restrict__ Bre, const u16* __restrict__ Bim, const u16* __restrict__ Cre,
                                                 const u16* __restrict__ Cim, const u16* __restrict__ logdt, const u16* __restrict__ cvec,
                                                 const u16* __restrict__ wmod, char* __restrict__ ws) {
  __shared__ __attribute__((aligned(16))) char smem[64 * TROW + 2624];
  const int tid = threadIdx.x, lane = tid & 63, wid = tid >> 6;
  int b = blockIdx.x;
  if (b < K1_MOD) {
    const int cb = b % 54, kq = b / 54, cc = tid & 7, rg = tid >> 3;
    u32x4 w[6]; u16 cv[6];
#pragma unroll
    for (int i = 0; i < 6; ++i) {
      const int row = 192 * kq + rg + 32 * i;
      w[i] = *(const u32x4*)(wmod + (size_t)row * MODN + 64 * cb + 8 * cc);
      cv[i] = cvec[row];
    }
    float acc[8] = {0, 0, 0, 0, 0, 0, 0, 0};
#pragma unroll
    for (int i = 0; i < 6; ++i) {
      const float s = bfr(silu_f(bf2f(cv[i])));
#pragma unroll
      for (int j = 0; j < 4; ++j) { acc[2 * j] += s * bflo(w[i][j]); acc[2 * j + 1] += s * bfhi(w[i][j]); }
    }
#pragma unroll
    for (int j = 0; j < 8; ++j) acc[j] = sum_hi3(acc[j]);
    float* red = (float*)smem;
    if (lane < 8) {
      *(f32x4*)(red + wid * 64 + lane * 8) = f32x4{acc[0], acc[1], acc[2], acc[3]};
      *(f32x4*)(red + wid * 64 + lane * 8 + 4) = f32x4{acc[4], acc[5], acc[6], acc[7]};
    }
    __syncthreads();
    if (tid < 64) ((float*)(ws + WS_MODP))[kq * MODN + 64 * cb + tid] = (red[tid] + red[64 + tid]) + (red[128 + tid] + red[192 + tid]);
    return;
  }
  b -= K1_MOD;
  if (b < K1_LN) {
    u16* tile = (u16*)smem;
    u32 v[2][4]; u16 t[2];
#pragma unroll
    for (int r = 0; r < 2; ++r) {
      const u16* row = x + (size_t)(8 * b + 2 * wid + r) * D;
#pragma unroll
      for (int i = 0; i < 4; ++i) v[r][i] = *(const u32*)(row + 2 * (lane + 64 * i));
      t[r] = row[512 + lane];
    }
#pragma unroll
    for (int r = 0; r < 2; ++r) {
      float e[9];
#pragma unroll
      for (int i = 0; i < 4; ++i) { e[2 * i] = bflo(v[r][i]); e[2 * i + 1] = bfhi(v[r][i]); }
      e[8] = bf2f(t[r]);
      float s = 0;
#pragma unroll
      for (int i = 0; i < 9; ++i) s += e[i];
      const float mean = wave_sum(s) * (1.0f / D);
      float q = 0;
#pragma unroll
      for (int i = 0; i < 9; ++i) { const float dlt = e[i] - mean; q += dlt * dlt; }
      const float var = wave_sum(q) * (1.0f / D);
      const float m = bfr(mean);
      const float rs = __builtin_amdgcn_rcpf(bfr(__builtin_amdgcn_sqrtf(bfr(bfr(var) + 1e-5f))));
      const int tok = 2 * wid + r;
#pragma unroll
      for (int i = 0; i < 4; ++i) {
        const u32 dd = pk_bf(e[2 * i] - m, e[2 * i + 1] - m);
        const u32 o = pk_bf(bflo(dd) * rs, bfhi(dd) * rs);
        tile[(2 * (lane + 64 * i)) * 8 + tok] = (u16)o;
        tile[(2 * (lane + 64 * i) + 1) * 8 + tok] = (u16)(o >> 16);
      }
      tile[(512 + lane) * 8 + tok] = (u16)pk_bf(bfr(e[8] - m) * rs, 0.f);
    }
    __syncthreads();
    u32x4* dst = (u32x4*)(ws + WS_XHT) + (size_t)b * D;
#pragma unroll
    for (int i = 0; i < 3; ++i) {
      const int c = tid + 256 * i;
      if (c < D) dst[c] = *(const u32x4*)(tile + c * 8);
    }
    return;
  }
  b -= K1_LN;
  {
    const int n = tid & 127, half = __builtin_amdgcn_readfirstlane(tid >> 7);
    const int idx = b * NM + n;
    const float are = bf2f(Are[idx]), aim = bf2f(Aim[idx]), bre = bf2f(Bre[idx]), bim = bf2f(Bim[idx]);
    const float cre = bf2f(Cre[idx]), cim = bf2f(Cim[idx]);
    const float dt = bfr(__builtin_amdgcn_exp2f(1.4426950408889634f * bf2f(logdt[b])));
    const float al2 = (dt * are) * 1.4426950408889634f, brev = (dt * aim) * 0.15915494309189535f;
    cplx c = cmul({cre, cim}, {dt * bre, dt * bim});
    const float cm = fmaxf(fabsf(c.re), fabsf(c.im));
    int he = (__builtin_amdgcn_frexp_expf(cm)) >> 1;
    c.re = __builtin_amdgcn_ldexpf(c.re, -he); c.im = __builtin_amdgcn_ldexpf(c.im, -he);
    char* Ut = smem + n * 4;
    char* Vt = smem + 48 * TROW + n * 4;
#define PUT_U(row, z) (*(u32*)(Ut + (row) * TROW) = pk_h((z).re, -(z).im))
#define PUT_V(row, z) (*(u32*)(Vt + (row) * TROW) = pk_h((z).re, (z).im))
    const cplx z128 = cexpl(al2, brev, 128.f);
    if (half == 0) {
      const cplx z8 = cexpl(al2, brev, 8.f);
      cplx v = {__builtin_amdgcn_ldexpf(1.0f, he), 0.f};
#pragma unroll
      for (int j = 0; j < 16; ++j) { PUT_V(j, v); if (j < 15) v = cmul(v, z8); }
      cplx u = cmul(c, cexpl(al2, brev, 1024.f));
#pragma unroll
      for (int j = 0; j < 9; ++j) { PUT_U(32 + j, u); if (j < 8) u = cmul(u, z128); }
      cplx bd = {fabsf(c.re) + fabsf(c.im), 0.f};
      PUT_U(41, bd);
    } else {
      const cplx z1 = cexpl(al2, brev, 1.f);
      cplx u[8];
      u[0] = c;
#pragma unroll
      for (int j = 1; j < 8; ++j) u[j] = cmul(u[j - 1], z1);
#pragma unroll
      for (int j = 0; j < 8; ++j) { PUT_U(j, u[j]); u[j] = cmul(u[j], z128); PUT_U(8 + j, u[j]); }
#pragma unroll
      for (int s = 0; s < 2; ++s)
#pragma unroll
        for (int j = 0; j < 4; ++j) { u[2 * j] = cmul(u[2 * j], z128); PUT_U(16 + 4 * s + j, u[2 * j]); }
#pragma unroll
      for (int s = 0; s < 4; ++s)
#pragma unroll
        for (int j = 0; j < 2; ++j) { u[4 * j] = cmul(u[4 * j], z128); PUT_U(24 + 2 * s + j, u[4 * j]); }
    }
#undef PUT_U
#undef PUT_V
    __syncthreads();
    float* Kc = (float*)(smem + 64 * TROW);
    if (wid < 3) {
      const int r = lane & 15, g = lane >> 4;
      f32x4 acc = {0, 0, 0, 0};
#pragma unroll
      for (int ks = 0; ks < 8; ++ks) {
        const f16x8 a = *(const f16x8*)(smem + (16 * wid + r) * TROW + ks * 64 + g * 16);
        const f16x8 v = *(const f16x8*)(smem + (48 + r) * TROW + ks * 64 + g * 16);
        acc = __builtin_amdgcn_mfma_f32_16x16x32_f16(a, v, acc, 0, 0, 0);
      }
#pragma unroll
      for (int e = 0; e < 4; ++e) {
        const int row = 4 * g + e;
        int o;
        if (wid == 0) o = (row < 8 ? row : 120 + row) + 8 * r;
        else if (wid == 1) o = row < 8 ? 256 + 64 * (row >> 2) + (row & 3) + 4 * r : 384 + 32 * ((row - 8) >> 1) + (row & 1) + 2 * r;
        else o = row < 8 ? 512 + 16 * row + r : ((row < 10 && r == 0) ? 632 + row : 650);
        Kc[o] = acc[e];
      }
    }
    __syncthreads();
    {
      const int q = tid;
      float k[8];
      if (q < 32) {
#pragma unroll
        for (int e = 0; e < 8; ++e) k[e] = Kc[8 * q + e];
      } else if (q < 64) {
        const float* p = Kc + 256 + 4 * (q - 32);
        k[0] = k[1] = p[0]; k[2] = p[1]; k[3] = k[4] = k[5] = p[2]; k[6] = p[3]; k[7] = p[4];
      } else if (q < 128) {
        const float* p = Kc + 384 + 2 * (q - 64);
        k[0] = k[1] = k[2] = p[0]; k[3] = k[4] = k[5] = p[1]; k[6] = k[7] = p[2];
      } else {
        const float* p = Kc + 512 + (q - 128);
        k[0] = k[1] = k[2] = k[3] = p[0]; k[5] = k[6] = k[7] = p[1]; k[4] = (q & 1) ? p[1] : p[0];
      }
      const int se = 14 - __builtin_amdgcn_frexp_expf(Kc[641]);
      u32x4 o;
#pragma unroll
      for (int e = 0; e < 4; ++e) o[e] = pk_h(__builtin_amdgcn_ldexpf(k[7 - 2 * e], se), __builtin_amdgcn_ldexpf(k[6 - 2 * e], se));
      ((u32x4*)(ws + WS_KX))[(size_t)b * 256 + q] = o;
      if (tid == 0) ((int*)(ws + WS_KSC))[b] = se;
    }
  }
}

constexpr int KCP = 4160;
constexpr int K2_H = 4 * KCP;
constexpr int K2_RED = K2_H + 4608;

__global__ __launch_bounds__(256) void k2_kernel(const u16* __restrict__ dskip, char* __restrict__ ws) {
  __shared__ __attribute__((aligned(16))) char smem[K2_RED + 24 * 1024];
  const int tid = threadIdx.x, lane = tid & 63, wid = __builtin_amdgcn_readfirstlane(tid >> 6);
  const int d = (blockIdx.x & 7) * 72 + (blockIdx.x >> 3);
  const u32x4* kx = (const u32x4*)(ws + WS_KX) + (size_t)d * 256;
  const u32x4 kA = kx[tid];
  u32x4 kB = kx[tid > 0 ? tid - 1 : 0];
  const u32x4 xh = ((const u32x4*)(ws + WS_XHT))[(size_t)tid * D + d];
  const float* mp = (const float*)(ws + WS_MODP);
  float md[6];
#pragma unroll
  for (int i = 0; i < 6; ++i) md[i] = bfr(mp[i * D + d] + mp[MODN + i * D + d] + mp[2 * MODN + i * D + d]);
  md[1] = bfr(1.0f + md[1]); md[4] = bfr(1.0f + md[4]);
  const int se = ((const int*)(ws + WS_KSC))[d];
  const float dk = bf2f(dskip[d]);
  if (tid == 0) {
    float* mf = (float*)(ws + WS_MODF);
#pragma unroll
    for (int i = 0; i < 6; ++i) mf[i * D + d] = md[i];
  }
  const float sa = md[0], c1 = md[1], ga = md[2];
  const int he = 14 - __builtin_amdgcn_frexp_expf(24.5f * fabsf(c1) + fabsf(sa));
  if (tid == 0) kB = u32x4{0, 0, 0, 0};
  {
    char* p = smem + (255 - tid) * 16;
    const u32 e0 = __builtin_amdgcn_alignbit(kA[1], kA[0], 16), e1 = __builtin_amdgcn_alignbit(kA[2], kA[1], 16);
    const u32 e2 = __builtin_amdgcn_alignbit(kA[3], kA[2], 16), e3 = __builtin_amdgcn_alignbit(kB[0], kA[3], 16);
    const u32 e4 = __builtin_amdgcn_alignbit(kB[1], kB[0], 16);
    *(u32x4*)(p + 0 * KCP) = u32x4{e1, e2, e3, e4};
    *(u32x4*)(p + 1 * KCP) = u32x4{kA[1], kA[2], kA[3], kB[0]};
    *(u32x4*)(p + 2 * KCP) = u32x4{e0, e1, e2, e3};
    *(u32x4*)(p + 3 * KCP) = kA;
    if (tid < 8) {
      *(u32x4*)(smem + (tid & 3) * KCP + (256 + (tid >> 2)) * 16) = u32x4{0, 0, 0, 0};
    }
    u32x4 hh;
#pragma unroll
    for (int i = 0; i < 4; ++i) {
      const u32 m = pk_bf(bflo(xh[i]) * c1, bfhi(xh[i]) * c1);
      const u32 h = pk_bf(bflo(m) + sa, bfhi(m) + sa);
      hh[i] = pk_h(__builtin_amdgcn_ldexpf(bflo(h), he), __builtin_amdgcn_ldexpf(bfhi(h), he));
    }
    *(u32x4*)(smem + K2_H + 512 + tid * 16) = hh;
    if (tid < 32) *(u32x4*)(smem + K2_H + tid * 16) = u32x4{0, 0, 0, 0};
  }
  __syncthreads();
  const int r = lane & 15, g = lane >> 4;
  f32x4 acc[8];
#pragma unroll
  for (int i = 0; i < 8; ++i) acc[i] = f32x4{0, 0, 0, 0};
  const char* abase = smem + (r & 3) * KCP + 2 * (2044 + 8 - 8 * g - (r & 12));
  const char* bbase = smem + K2_H + 512 + 2 * (16 * r - 8 * g + 8);
#pragma unroll
  for (int cls = 0; cls < 2; ++cls) {
    const int i0 = 2 * wid + cls;
    const char* ap = abase - 64 * (7 - i0);
    const char* bp = bbase + 64 * (i0 - 7);
    f16x8 A[8], B[8];
#pragma unroll
    for (int m = 0; m < 8; ++m) {
      const u32x2 lo = *(const u32x2*)(ap - 512 * m), hi = *(const u32x2*)(ap - 512 * m + 8);
      A[m] = __builtin_bit_cast(f16x8, u32x4{lo[0], lo[1], hi[0], hi[1]});
      B[m] = *(const f16x8*)(bp + 512 * m);
    }
#pragma unroll
    for (int n = 0; n < 8; ++n)
#pragma unroll
      for (int k = 0; k <= n; ++k) acc[n] = __builtin_amdgcn_mfma_f32_16x16x32_f16(A[n - k], B[k], acc[n], 0, 0, 0);
  }
  f32x4* red = (f32x4*)(smem + K2_RED);
#pragma unroll
  for (int n = 0; n < 8; ++n) {
    const int o = n >> 1;
    if (o != wid) red[(n * 3 + (wid > o ? wid - 1 : wid)) * 64 + lane] = acc[n];
  }
  __syncthreads();
#pragma unroll
  for (int e = 0; e < 2; ++e) {
    const int n = 2 * wid + e;
    f32x4 s = wid == 0 ? acc[e] : wid == 1 ? acc[2 + e] : wid == 2 ? acc[4 + e] : acc[6 + e];
#pragma unroll
    for (int j = 0; j < 3; ++j) s += red[(n * 3 + j) * 64 + lane];
    const int t = 256 * n + 16 * r + 4 * g;
    const u32x2 hv = *(const u32x2*)(smem + K2_H + 512 + 2 * t);
    u32x2 o;
#pragma unroll
    for (int j = 0; j < 2; ++j) {
      const u32 hw = hv[j];
      const f16x2 hp = __builtin_bit_cast(f16x2, hw);
      const u32 y = pk_bf(__builtin_amdgcn_ldexpf(s[2 * j], -se - he), __builtin_amdgcn_ldexpf(s[2 * j + 1], -se - he));
      const u32 sk = pk_bf(__builtin_amdgcn_ldexpf((float)hp[0], -he) * dk, __builtin_amdgcn_ldexpf((float)hp[1], -he) * dk);
      const u32 ys = pk_bf(bflo(y) + bflo(sk), bfhi(y) + bfhi(sk));
      o[j] = pk_bf(ga * bflo(ys), ga * bfhi(ys));
    }
    *(u32x2*)(ws + WS_RT + ((size_t)(t >> 3) * D + d) * 16 + (t & 4) * 2) = o;
  }
}

__global__ __launch_bounds__(576) void k3_kernel(const u16* __restrict__ x, char* __restrict__ ws) {
  __shared__ float red[2][9][8];
  const int tid = threadIdx.x, lane = tid & 63, wid = tid >> 6;
  const int tq = (blockIdx.x & 7) * 32 + (blockIdx.x >> 3);
  const int tok = lane & 7, piece = tid >> 3, ks = piece >> 2, g = piece & 3;
  const int dA = 32 * ks + 4 * g, t = 8 * tq + tok;
  const u32x2 xa = *(const u32x2*)(x + (size_t)t * D + dA), xb = *(const u32x2*)(x + (size_t)t * D + dA + 16);
  const u16* rt = (const u16*)(ws + WS_RT) + ((size_t)tq * D + dA) * 8 + tok;
  u16 rv[8];
#pragma unroll
  for (int j = 0; j < 4; ++j) { rv[j] = rt[8 * j]; rv[4 + j] = rt[128 + 8 * j]; }
  const float* mf = (const float*)(ws + WS_MODF);
  const f32x4 sbA = *(const f32x4*)(mf + 3 * D + dA), sbB = *(const f32x4*)(mf + 3 * D + dA + 16);
  const f32x4 cbA = *(const f32x4*)(mf + 4 * D + dA), cbB = *(const f32x4*)(mf + 4 * D + dA + 16);
  u32 p[4];
  p[0] = pk_bf(bflo(xa[0]) + bf2f(rv[0]), bfhi(xa[0]) + bf2f(rv[1]));
  p[1] = pk_bf(bflo(xa[1]) + bf2f(rv[2]), bfhi(xa[1]) + bf2f(rv[3]));
  p[2] = pk_bf(bflo(xb[0]) + bf2f(rv[4]), bfhi(xb[0]) + bf2f(rv[5]));
  p[3] = pk_bf(bflo(xb[1]) + bf2f(rv[6]), bfhi(xb[1]) + bf2f(rv[7]));
  u16* x1 = (u16*)(ws + WS_X1) + (size_t)t * D + dA;
  *(u32x2*)x1 = u32x2{p[0], p[1]};
  *(u32x2*)(x1 + 16) = u32x2{p[2], p[3]};
  float e[8];
#pragma unroll
  for (int j = 0; j < 4; ++j) { e[2 * j] = bflo(p[j]); e[2 * j + 1] = bfhi(p[j]); }
  float s = ((e[0] + e[1]) + (e[2] + e[3])) + ((e[4] + e[5]) + (e[6] + e[7]));
  s = sum_hi3(s);
  if (lane < 8) red[0][wid][lane] = s;
  __syncthreads();
  float mean = 0;
#pragma unroll
  for (int w = 0; w < 9; ++w) mean += red[0][w][tok];
  mean *= (1.0f / D);
  float q = 0;
#pragma unroll
  for (int j = 0; j < 8; ++j) { const float dl = e[j] - mean; q += dl * dl; }
  q = sum_hi3(q);
  if (lane < 8) red[1][wid][lane] = q;
  __syncthreads();
  float var = 0;
#pragma unroll
  for (int w = 0; w < 9; ++w) var += red[1][w][tok];
  var *= (1.0f / D);
  const float m = bfr(mean);
  const float rs = __builtin_amdgcn_rcpf(bfr(__builtin_amdgcn_sqrtf(bfr(bfr(var) + 1e-5f))));
  u32x4 o;
#pragma unroll
  for (int j = 0; j < 4; ++j) {
    const float c0 = j < 2 ? cbA[2 * j] : cbB[2 * j - 4], c1 = j < 2 ? cbA[2 * j + 1] : cbB[2 * j - 3];
    const float s0 = j < 2 ? sbA[2 * j] : sbB[2 * j - 4], s1 = j < 2 ? sbA[2 * j + 1] : sbB[2 * j - 3];
    const u32 dd = pk_bf(e[2 * j] - m, e[2 * j + 1] - m);
    const u32 xh = pk_bf(bflo(dd) * rs, bfhi(dd) * rs);
    const u32 mm = pk_bf(bflo(xh) * c0, bfhi(xh) * c1);
    o[j] = pk_bf(bflo(mm) + s0, bfhi(mm) + s1);
  }
  ((u32x4*)(ws + WS_H2T))[((size_t)(tq >> 1) * 18 + ks) * 64 + 16 * g + 8 * (tq & 1) + tok] = o;
}

#define WAIT_V(n) asm volatile("s_waitcnt vmcnt(" #n ")" ::: "memory")
#define WAIT_L0() asm volatile("s_waitcnt lgkmcnt(0)" ::: "memory")
#define BAR() __builtin_amdgcn_s_barrier()
#define SCHED() __builtin_amdgcn_sched_barrier(0)
__device__ __forceinline__ void glds16(const void* g, char* l) {
  __builtin_amdgcn_global_load_lds((const GAS void*)g, (LAS void*)l, 16, 0, 0);
}
__device__ __forceinline__ bf16x8 tr_frag(const char* p, int o2) {
  const s16x4 a = __builtin_amdgcn_ds_read_tr16_b64_v4i16((LAS s16x4*)p);
  const s16x4 b = __builtin_amdgcn_ds_read_tr16_b64_v4i16((LAS s16x4*)(p + o2));
  return __builtin_bit_cast(bf16x8, __builtin_shufflevector(a, b, 0, 1, 2, 3, 4, 5, 6, 7));
}

constexpr int G1_W1 = 16384, G1_W3 = G1_W1 + 9216 + 16, G1_STAGE = 35072, G1_LDS = 4 * G1_STAGE;

__global__ __launch_bounds__(512, 2) void k4_kernel(const u16* __restrict__ w1, const u16* __restrict__ w3, char* __restrict__ ws) {
  extern __shared__ __attribute__((aligned(16))) char smem[];
  const int tid = threadIdx.x, lane = tid & 63, wid = __builtin_amdgcn_readfirstlane(tid >> 6);
  const int mt = (blockIdx.x >> 3) & 7, ft = (blockIdx.x & 7) * 4 + (blockIdx.x >> 6);
  const int wm = wid & 3, wn = wid >> 2;
  const char* tsrc = ws + WS_H2T + (size_t)(16 * mt + 2 * wid) * 18 * 1024 + lane * 16;
  const char* wsrc = (const char*)(wn ? w3 : w1) + (size_t)(8 * wm) * (F * 2) + ft * 288;
  const int x1 = 64 + lane, x2 = 128 + (lane & 15);
  const int o0 = (lane / 18) * (F * 2) + (lane % 18) * 16, o1 = (x1 / 18) * (F * 2) + (x1 % 18) * 16, o2 = (x2 / 18) * (F * 2) + (x2 % 18) * 16;
  const int tdst = 2 * wid * 1024, wdst = (wn ? G1_W3 : G1_W1) + wm * 2304;
  auto stage = [&](int p) {
    char* sb = smem + (p & 3) * G1_STAGE;
    glds16(tsrc + p * 1024, sb + tdst);
    glds16(tsrc + (18 + p) * 1024, sb + tdst + 1024);
    const char* wp = wsrc + (size_t)p * 32 * (F * 2);
    glds16(wp + o0, sb + wdst);
    glds16(wp + o1, sb + wdst + 1024);
    if (lane < 16) glds16(wp + o2, sb + wdst + 2048);
  };
  const int g = lane >> 4, q = (lane >> 2) & 3, pc = lane & 3;
  const int boff = wm * 4096 + lane * 16;
  const int aoff = G1_W1 + (4 * g + q) * 288 + wn * 144 + 8 * pc;
  const int moff = (pc < 2 ? G1_W1 : G1_W3) + (4 * g + q) * 288 + wn * 144 + 128 + 8 * (pc & 1);
  f32x4 acc[9][4];
#pragma unroll
  for (int i = 0; i < 9; ++i)
#pragma unroll
    for (int j = 0; j < 4; ++j) acc[i][j] = f32x4{0, 0, 0, 0};

  stage(0); stage(1); stage(2);
  WAIT_V(10); BAR();
  if (wn) BAR();
#pragma unroll
  for (int p = 0; p < 18; ++p) {
    const char* sb = smem + (p & 3) * G1_STAGE;
    bf16x8 A[9], B[4];
#pragma unroll
    for (int j = 0; j < 4; ++j) B[j] = *(const bf16x8*)(sb + boff + j * 1024);
#pragma unroll
    for (int i = 0; i < 4; ++i) {
      A[i] = tr_frag(sb + aoff + 32 * i, 16 * 288);
      A[4 + i] = tr_frag(sb + aoff + (G1_W3 - G1_W1) + 32 * i, 16 * 288);
    }
    A[8] = tr_frag(sb + moff, 16 * 288);
    if (p + 3 < 18) stage(p + 3);
    if (p < 15) WAIT_V(10); else if (p == 15) WAIT_V(5); else if (p == 16) WAIT_V(0);
    WAIT_L0(); SCHED();
    BAR();
    __builtin_amdgcn_s_setprio(1);
#pragma unroll
    for (int i = 0; i < 9; ++i)
#pragma unroll
      for (int j = 0; j < 4; ++j) acc[i][j] = __builtin_amdgcn_mfma_f32_16x16x32_bf16(A[i], B[j], acc[i][j], 0, 0, 0);
    __builtin_amdgcn_s_setprio(0);
    SCHED();
    if (p < 17 || !wn) BAR();
  }
  const int P = ft >> 1, qq = 2 * (ft & 1) + wn;
  char* gbase = ws + WS_GT + ((size_t)(16 * mt + 4 * wm) * 144 + 9 * P) * 1024;
  auto glu2 = [&](float g0, float g1, float u0, float u1) -> u32 {
    const u32 gg = pk_bf(g0, g1), uu = pk_bf(u0, u1);
    const u32 ss = pk_bf(silu_f(bflo(gg)), silu_f(bfhi(gg)));
    return pk_bf(bflo(ss) * bflo(uu), bfhi(ss) * bfhi(uu));
  };
#pragma unroll
  for (int j = 0; j < 4; ++j)
#pragma unroll
    for (int s = 0; s < 2; ++s) {
      u32x4 o;
#pragma unroll
      for (int h = 0; h < 2; ++h) {
        const f32x4 a = acc[2 * s + h][j], u = acc[4 + 2 * s + h][j];
        o[2 * h] = glu2(a[0], a[1], u[0], u[1]);
        o[2 * h + 1] = glu2(a[2], a[3], u[2], u[3]);
      }
      *(u32x4*)(gbase + (size_t)j * 144 * 1024 + (2 * qq + s) * 1024 + lane * 16) = o;
    }
#pragma unroll
  for (int jp = 0; jp < 2; ++jp) {
    float a[4], u[4];
#pragma unroll
    for (int e = 0; e < 4; ++e) {
      const float m0 = acc[8][2 * jp][e], m1 = acc[8][2 * jp + 1][e];
      auto r = __builtin_amdgcn_permlane32_swap(__builtin_bit_cast(u32, m0), __builtin_bit_cast(u32, m1), false, false);
      a[e] = __builtin_bit_cast(float, (u32)r[0]); u[e] = __builtin_bit_cast(float, (u32)r[1]);
    }
    const u32x2 o = {glu2(a[0], a[1], u[0], u[1]), glu2(a[2], a[3], u[2], u[3])};
    const int j = 2 * jp + (lane >> 5);
    *(u32x2*)(gbase + (size_t)j * 144 * 1024 + 8 * 1024 + (16 * (2 * (qq & 1) + (g & 1)) + (lane & 15)) * 16 + 8 * (qq >> 1)) = o;
  }
}

constexpr int G2_LDS = 16 * 9 * 1024;

template <int TT>
__device__ __forceinline__ void k5_body(const u16* __restrict__ w2, char* __restrict__ ws, u16* __restrict__ out, char* smem, int tile0, int cb) {
  constexpr int SLAB = (TT + 4) * 1024;
  const int tid = threadIdx.x, lane = tid & 63, wid = __builtin_amdgcn_readfirstlane(tid >> 6);
  const int g = lane >> 4, q = (lane >> 2) & 3, pc = lane & 3;
  char* ring = smem + wid * 4 * SLAB;
  const char* gsrc = ws + WS_GT + ((size_t)tile0 * 144 + 36 * wid) * 1024 + lane * 16;
  const char* wsrc = (const char*)w2 + (size_t)(1152 * wid) * (D * 2) + cb * 128 + (lane >> 3) * (D * 2) + (((lane & 7) ^ (((lane >> 4) & 3) << 1)) * 16);
  auto stage = [&](int pp, int i, int slot) {
    char* sb = ring + slot * SLAB;
#pragma unroll
    for (int j = 0; j < TT; ++j) glds16(gsrc + ((size_t)j * 144 + 9 * pp + i) * 1024, sb + j * 1024);
    const int f0 = 288 * pp + (i < 8 ? 72 * (i >> 1) + 32 * (i & 1) : 64);
#pragma unroll
    for (int j = 0; j < 4; ++j) glds16(wsrc + (size_t)(f0 + (i < 8 ? 8 : 72) * j) * (D * 2), sb + TT * 1024 + j * 1024);
  };
  const int sw = (2 * g + (q >> 1)) & 3;
  int aoff[4];
#pragma unroll
  for (int c = 0; c < 4; ++c) aoff[c] = TT * 1024 + (4 * g + q) * 128 + (((2 * (c ^ sw)) + (pc >> 1)) * 16) + 8 * (pc & 1);
  f32x4 acc[4][TT];
#pragma unroll
  for (int c = 0; c < 4; ++c)
#pragma unroll
    for (int j = 0; j < TT; ++j) acc[c][j] = f32x4{0, 0, 0, 0};
  stage(0, 0, 0); stage(0, 1, 1); stage(0, 2, 2);
#pragma unroll
  for (int s = 0; s < 36; ++s) {
    if (s + 3 < 36) stage((s + 3) / 9, (s + 3) % 9, (s + 3) & 3);
    const int left = (s + 3 < 36 ? 3 : 35 - s) * (TT + 4);
    asm volatile("s_waitcnt vmcnt(%0)" ::"i"(left) : "memory");
    const char* sb = ring + (s & 3) * SLAB;
    bf16x8 A[4], B[TT];
#pragma unroll
    for (int j = 0; j < TT; ++j) B[j] = *(const bf16x8*)(sb + j * 1024 + lane * 16);
#pragma unroll
    for (int c = 0; c < 4; ++c) A[c] = tr_frag(sb + aoff[c], 2048);
    WAIT_L0(); SCHED();
#pragma unroll
    for (int c = 0; c < 4; ++c)
#pragma unroll
      for (int j = 0; j < TT; ++j) acc[c][j] = __builtin_amdgcn_mfma_f32_16x16x32_bf16(A[c], B[j], acc[c][j], 0, 0, 0);
    SCHED();
  }
  const int col = 64 * cb + 16 * wid + 4 * g;
  const f32x4 gb = *(const f32x4*)((const float*)(ws + WS_MODF) + 5 * D + col);
  u32x2 xr[TT];
#pragma unroll
  for (int j = 0; j < TT; ++j) xr[j] = *(const u32x2*)((const u16*)(ws + WS_X1) + (size_t)(16 * (tile0 + j) + (lane & 15)) * D + col);
  __syncthreads();
  f32x4* red = (f32x4*)smem;
#pragma unroll
  for (int c = 0; c < 4; ++c)
    if (c != wid) {
#pragma unroll
      for (int j = 0; j < TT; ++j) red[((c * 3 + (wid > c ? wid - 1 : wid)) * TT + j) * 64 + lane] = acc[c][j];
    }
  __syncthreads();
#pragma unroll
  for (int j = 0; j < TT; ++j) {
    f32x4 s = wid == 0 ? acc[0][j] : wid == 1 ? acc[1][j] : wid == 2 ? acc[2][j] : acc[3][j];
#pragma unroll
    for (int k = 0; k < 3; ++k) s += red[((wid * 3 + k) * TT + j) * 64 + lane];
    u32x2 o;
#pragma unroll
    for (int h = 0; h < 2; ++h) {
      const u32 y = pk_bf(s[2 * h], s[2 * h + 1]);
      const u32 z = pk_bf(gb[2 * h] * bflo(y), gb[2 * h + 1] * bfhi(y));
      o[h] = pk_bf(bflo(xr[j][h]) + bflo(z), bfhi(xr[j][h]) + bfhi(z));
    }
    *(u32x2*)(out + (size_t)(16 * (tile0 + j) + (lane & 15)) * D + col) = o;
  }
}

__global__ __launch_bounds__(256, 1) void k5_kernel(const u16* __restrict__ w2, char* __restrict__ ws, u16* __restrict__ out) {
  extern __shared__ __attribute__((aligned(16))) char smem[];
  const int xcd = blockIdx.x & 7, off = blockIdx.x >> 3;
  const int id = (xcd < 4 ? xcd * 32 : 128 + (xcd - 4) * 31) + off;
  const int rb = id / 9, cb = id % 9;
  if (rb < 16) k5_body<5>(w2, ws, out, smem, 5 * rb, cb);
  else k5_body<4>(w2, ws, out, smem, 80 + 4 * (rb - 16), cb);
}

extern "C" void kernel_launch(void* const* d_in, const int* in_sizes, int n_in, void* d_out, int out_size, void* d_ws, size_t ws_size,
                              hipStream_t stream) {
  const u16* u = (const u16*)d_in[0];
  char* ws = (char*)d_ws;
  (void)hipFuncSetAttribute((const void*)k4_kernel, hipFuncAttributeMaxDynamicSharedMemorySize, G1_LDS);
  (void)hipFuncSetAttribute((const void*)k5_kernel, hipFuncAttributeMaxDynamicSharedMemorySize, G2_LDS);
  k1_kernel<<<K1_MOD + K1_LN + K1_KG, 256, 0, stream>>>(u, (const u16*)d_in[1], (const u16*)d_in[2], (const u16*)d_in[3], (const u16*)d_in[4],
                                                        (const u16*)d_in[5], (const u16*)d_in[6], (const u16*)d_in[7], (const u16*)d_in[13],
                                                        (const u16*)d_in[14], ws);
  k2_kernel<<<D, 256, 0, stream>>>((const u16*)d_in[8], ws);
  k3_kernel<<<256, 576, 0, stream>>>(u, ws);
  k4_kernel<<<256, 512, G1_LDS, stream>>>((const u16*)d_in[10], (const u16*)d_in[11], ws);
  k5_kernel<<<252, 256, G2_LDS, stream>>>((const u16*)d_in[12], ws, (u16*)d_out);
}
```

```cpp
#include <hip/hip_runtime.h>
#include <hip/hip_bf16.h>

typedef unsigned short u16;
typedef unsigned int u32;
typedef __attribute__((ext_vector_type(2))) float f32x2;
typedef __attribute__((ext_vector_type(4))) float f32x4;
typedef __attribute__((ext_vector_type(2))) u32 u32x2;
typedef __attribute__((ext_vector_type(4))) u32 u32x4;
typedef __attribute__((ext_vector_type(4))) short s16x4;
typedef __attribute__((ext_vector_type(8))) short s16x8;
typedef __attribute__((ext_vector_type(2))) __bf16 bf16x2;
typedef __attribute__((ext_vector_type(8))) __bf16 bf16x8;
typedef __attribute__((ext_vector_type(2))) _Float16 f16x2;
typedef __attribute__((ext_vector_type(8))) _Float16 f16x8;
#define LAS __attribute__((address_space(3)))
#define GAS __attribute__((address_space(1)))

constexpr int L = 2048, D = 576, NM = 128, F = 4608, MODN = 6 * D;
constexpr int KSPLIT = 3;

constexpr size_t WS_MODP = 0;
constexpr size_t WS_MODF = 65536;
constexpr size_t WS_KSC  = WS_MODF + 16384;
constexpr size_t WS_XHT  = 131072;
constexpr size_t ACT_B   = (size_t)L * D * 2;
constexpr size_t WS_KX   = WS_XHT + ACT_B;
constexpr size_t WS_RT   = WS_KX + ACT_B;
constexpr size_t WS_X1   = WS_RT + ACT_B;
constexpr size_t WS_H2T  = WS_X1 + ACT_B;
constexpr size_t WS_GT   = WS_H2T + ACT_B;

#define SCHED() __builtin_amdgcn_sched_barrier(0)
#define STAMP_BEGIN()
#define STAMP_MID(i)
#define STAMP_END(k)
__device__ __forceinline__ float bf2f(u16 v) { return __builtin_bit_cast(float, (u32)v << 16); }
__device__ __forceinline__ float bflo(u32 v) { return __builtin_bit_cast(float, v << 16); }
__device__ __forceinline__ float bfhi(u32 v) { return __builtin_bit_cast(float, v & 0xffff0000u); }
__device__ __forceinline__ u32 pk_bf(float a, float b) { f32x2 v = {a, b}; return __builtin_bit_cast(u32, __builtin_convertvector(v, bf16x2)); }
__device__ __forceinline__ u32 pk_h(float a, float b) { f32x2 v = {a, b}; return __builtin_bit_cast(u32, __builtin_convertvector(v, f16x2)); }
__device__ __forceinline__ float bfr(float a) { return bflo(pk_bf(a, a)); }

template <int CTRL> __device__ __forceinline__ float dpp(float v) {
  return __builtin_bit_cast(float, __builtin_amdgcn_update_dpp(0, __builtin_bit_cast(int, v), CTRL, 0xf, 0xf, true));
}
__device__ __forceinline__ float sum16x(float v) {
  u32 u = __builtin_bit_cast(u32, v);
  auto r = __builtin_amdgcn_permlane16_swap(u, u, false, false);
  return __builtin_bit_cast(float, (u32)r[0]) + __builtin_bit_cast(float, (u32)r[1]);
}
__device__ __forceinline__ float sum32x(float v) {
  u32 u = __builtin_bit_cast(u32, v);
  auto r = __builtin_amdgcn_permlane32_swap(u, u, false, false);
  return __builtin_bit_cast(float, (u32)r[0]) + __builtin_bit_cast(float, (u32)r[1]);
}
__device__ __forceinline__ float wave_sum(float v) {
  v += dpp<0xB1>(v);
  v += dpp<0x4E>(v);
  v += dpp<0x141>(v);
  v += dpp<0x140>(v);
  return sum32x(sum16x(v));
}
__device__ __forceinline__ float sum_hi3(float v) {
  v += dpp<0x128>(v);
  return sum32x(sum16x(v));
}
__device__ __forceinline__ float silu_f(float g) { return g * __builtin_amdgcn_rcpf(1.0f + __builtin_amdgcn_exp2f(-1.4426950408889634f * g)); }

constexpr int K1_MOD = 54 * KSPLIT, K1_LN = 256, K1_KG = D;
constexpr int TROW = 528;

struct cplx { float re, im; };
__device__ __forceinline__ cplx cmul(cplx a, cplx b) { return {a.re * b.re - a.im * b.im, a.re * b.im + a.im * b.re}; }
__device__ __forceinline__ cplx cexpl(float al2, float brev, float l) {
  float m = __builtin_amdgcn_exp2f(al2 * l);
  float ph = __builtin_amdgcn_fractf(brev * l);
  return {m * __builtin_amdgcn_cosf(ph), m * __builtin_amdgcn_sinf(ph)};
}

__global__ __launch_bounds__(256) void k1_kernel(const u16* __restrict__ x, const u16* __restrict__ Are, const u16* __restrict__ Aim,
                                                 const u16* __restrict__ Bre, const u16* __restrict__ Bim, const u16* __restrict__ Cre,
                                                 const u16* __restrict__ Cim, const u16* __restrict__ logdt, const u16* __restrict__ cvec,
                                                 const u16* __restrict__ wmod, char* __restrict__ ws) {
  __shared__ __attribute__((aligned(16))) char smem[64 * TROW + 2624];
  STAMP_BEGIN();
  const int tid = threadIdx.x, lane = tid & 63, wid = tid >> 6;
  int b = blockIdx.x;
  if (b < K1_MOD) {
    const int cb = b % 54, kq = b / 54, cc = tid & 7, rg = tid >> 3;
    u32x4 w[6]; u16 cv[6];
#pragma unroll
    for (int i = 0; i < 6; ++i) {
      const int row = 192 * kq + rg + 32 * i;
      w[i] = *(const u32x4*)(wmod + (size_t)row * MODN + 64 * cb + 8 * cc);
      cv[i] = cvec[row];
    }
    float acc[8] = {0, 0, 0, 0, 0, 0, 0, 0};
#pragma unroll
    for (int i = 0; i < 6; ++i) {
      const float s = bfr(silu_f(bf2f(cv[i])));
#pragma unroll
      for (int j = 0; j < 4; ++j) { acc[2 * j] += s * bflo(w[i][j]); acc[2 * j + 1] += s * bfhi(w[i][j]); }
    }
#pragma unroll
    for (int j = 0; j < 8; ++j) acc[j] = sum_hi3(acc[j]);
    float* red = (float*)smem;
    if (lane < 8) {
      *(f32x4*)(red + wid * 64 + lane * 8) = f32x4{acc[0], acc[1], acc[2], acc[3]};
      *(f32x4*)(red + wid * 64 + lane * 8 + 4) = f32x4{acc[4], acc[5], acc[6], acc[7]};
    }
    __syncthreads();
    if (tid < 64) ((float*)(ws + WS_MODP))[kq * MODN + 64 * cb + tid] = (red[tid] + red[64 + tid]) + (red[128 + tid] + red[192 + tid]);
    STAMP_END(0);
    return;
  }
  b -= K1_MOD;
  if (b < K1_LN) {
    u16* tile = (u16*)smem;
    u32 v[2][4]; u16 t[2];
#pragma unroll
    for (int r = 0; r < 2; ++r) {
      const u16* row = x + (size_t)(8 * b + 2 * wid + r) * D;
#pragma unroll
      for (int i = 0; i < 4; ++i) v[r][i] = *(const u32*)(row + 2 * (lane + 64 * i));
      t[r] = row[512 + lane];
    }
#pragma unroll
    for (int r = 0; r < 2; ++r) {
      float e[9];
#pragma unroll
      for (int i = 0; i < 4; ++i) { e[2 * i] = bflo(v[r][i]); e[2 * i + 1] = bfhi(v[r][i]); }
      e[8] = bf2f(t[r]);
      float s = 0;
#pragma unroll
      for (int i = 0; i < 9; ++i) s += e[i];
      const float mean = wave_sum(s) * (1.0f / D);
      float q = 0;
#pragma unroll
      for (int i = 0; i < 9; ++i) { const float dlt = e[i] - mean; q += dlt * dlt; }
      const float var = wave_sum(q) * (1.0f / D);
      const float m = bfr(mean);
      const float rs = __builtin_amdgcn_rcpf(bfr(__builtin_amdgcn_sqrtf(bfr(bfr(var) + 1e-5f))));
      const int tok = 2 * wid + r;
#pragma unroll
      for (int i = 0; i < 4; ++i) {
        const u32 dd = pk_bf(e[2 * i] - m, e[2 * i + 1] - m);
        const u32 o = pk_bf(bflo(dd) * rs, bfhi(dd) * rs);
        tile[(2 * (lane + 64 * i)) * 8 + tok] = (u16)o;
        tile[(2 * (lane + 64 * i) + 1) * 8 + tok] = (u16)(o >> 16);
      }
      tile[(512 + lane) * 8 + tok] = (u16)pk_bf(bfr(e[8] - m) * rs, 0.f);
    }
    __syncthreads();
    u32x4* dst = (u32x4*)(ws + WS_XHT) + (size_t)b * D;
#pragma unroll
    for (int i = 0; i < 3; ++i) {
      const int c = tid + 256 * i;
      if (c < D) dst[c] = *(const u32x4*)(tile + c * 8);
    }
    STAMP_END(0);
    return;
  }
  b -= K1_LN;
  {
    const int n = tid & 127, half = __builtin_amdgcn_readfirstlane(tid >> 7);
    const int idx = b * NM + n;
    const float are = bf2f(Are[idx]), aim = bf2f(Aim[idx]), bre = bf2f(Bre[idx]), bim = bf2f(Bim[idx]);
    const float cre = bf2f(Cre[idx]), cim = bf2f(Cim[idx]);
    const float dt = bfr(__builtin_amdgcn_exp2f(1.4426950408889634f * bf2f(logdt[b])));
    const float al2 = (dt * are) * 1.4426950408889634f, brev = (dt * aim) * 0.15915494309189535f;
    cplx c = cmul({cre, cim}, {dt * bre, dt * bim});
    const float cm = fmaxf(fabsf(c.re), fabsf(c.im));
    int he = (__builtin_amdgcn_frexp_expf(cm)) >> 1;
    c.re = __builtin_amdgcn_ldexpf(c.re, -he); c.im = __builtin_amdgcn_ldexpf(c.im, -he);
    char* Ut = smem + n * 4;
    char* Vt = smem + 48 * TROW + n * 4;
#define PUT_U(row, z) (*(u32*)(Ut + (row) * TROW) = pk_h((z).re, -(z).im))
#define PUT_V(row, z) (*(u32*)(Vt + (row) * TROW) = pk_h((z).re, (z).im))
    const cplx z128 = cexpl(al2, brev, 128.f);
    if (half == 0) {
      const cplx z8 = cexpl(al2, brev, 8.f);
      cplx v = {__builtin_amdgcn_ldexpf(1.0f, he), 0.f};
#pragma unroll
      for (int j = 0; j < 16; ++j) { PUT_V(j, v); if (j < 15) v = cmul(v, z8); }
      cplx u = cmul(c, cexpl(al2, brev, 1024.f));
#pragma unroll
      for (int j = 0; j < 9; ++j) { PUT_U(32 + j, u); if (j < 8) u = cmul(u, z128); }
      cplx bd = {fabsf(c.re) + fabsf(c.im), 0.f};
      PUT_U(41, bd);
    } else {
      const cplx z1 = cexpl(al2, brev, 1.f);
      cplx u[8];
      u[0] = c;
#pragma unroll
      for (int j = 1; j < 8; ++j) u[j] = cmul(u[j - 1], z1);
#pragma unroll
      for (int j = 0; j < 8; ++j) { PUT_U(j, u[j]); u[j] = cmul(u[j], z128); PUT_U(8 + j, u[j]); }
#pragma unroll
      for (int s = 0; s < 2; ++s)
#pragma unroll
        for (int j = 0; j < 4; ++j) { u[2 * j] = cmul(u[2 * j], z128); PUT_U(16 + 4 * s + j, u[2 * j]); }
#pragma unroll
      for (int s = 0; s < 4; ++s)
#pragma unroll
        for (int j = 0; j < 2; ++j) { u[4 * j] = cmul(u[4 * j], z128); PUT_U(24 + 2 * s + j, u[4 * j]); }
    }
#undef PUT_U
#undef PUT_V
    __syncthreads();
    float* Kc = (float*)(smem + 64 * TROW);
    if (wid < 3) {
      const int r = lane & 15, g = lane >> 4;
      f32x4 acc = {0, 0, 0, 0};
#pragma unroll
      for (int ks = 0; ks < 8; ++ks) {
        const f16x8 a = *(const f16x8*)(smem + (16 * wid + r) * TROW + ks * 64 + g * 16);
        const f16x8 v = *(const f16x8*)(smem + (48 + r) * TROW + ks * 64 + g * 16);
        acc = __builtin_amdgcn_mfma_f32_16x16x32_f16(a, v, acc, 0, 0, 0);
      }
#pragma unroll
      for (int e = 0; e < 4; ++e) {
        const int row = 4 * g + e;
        int o;
        if (wid == 0) o = (row < 8 ? row : 120 + row) + 8 * r;
        else if (wid == 1) o = row < 8 ? 256 + 64 * (row >> 2) + (row & 3) + 4 * r : 384 + 32 * ((row - 8) >> 1) + (row & 1) + 2 * r;
        else o = row < 8 ? 512 + 16 * row + r : ((row < 10 && r == 0) ? 632 + row : 650);
        Kc[o] = acc[e];
      }
    }
    __syncthreads();
    {
      const int q = tid;
      float k[8];
      if (q < 32) {
#pragma unroll
        for (int e = 0; e < 8; ++e) k[e] = Kc[8 * q + e];
      } else if (q < 64) {
        const float* p = Kc + 256 + 4 * (q - 32);
        k[0] = k[1] = p[0]; k[2] = p[1]; k[3] = k[4] = k[5] = p[2]; k[6] = p[3]; k[7] = p[4];
      } else if (q < 128) {
        const float* p = Kc + 384 + 2 * (q - 64);
        k[0] = k[1] = k[2] = p[0]; k[3] = k[4] = k[5] = p[1]; k[6] = k[7] = p[2];
      } else {
        const float* p = Kc + 512 + (q - 128);
        k[0] = k[1] = k[2] = k[3] = p[0]; k[5] = k[6] = k[7] = p[1]; k[4] = (q & 1) ? p[1] : p[0];
      }
      const int se = 14 - __builtin_amdgcn_frexp_expf(Kc[641]);
      u32x4 o;
#pragma unroll
      for (int e = 0; e < 4; ++e) o[e] = pk_h(__builtin_amdgcn_ldexpf(k[7 - 2 * e], se), __builtin_amdgcn_ldexpf(k[6 - 2 * e], se));
      ((u32x4*)(ws + WS_KX))[(size_t)b * 256 + q] = o;
      if (tid == 0) ((int*)(ws + WS_KSC))[b] = se;
    }
    STAMP_END(0);
  }
}

constexpr int KCP = 4160;
constexpr int K2_H = 4 * KCP;
constexpr int K2_RED = K2_H + 4608;

__global__ __launch_bounds__(256) void k2_kernel(const u16* __restrict__ dskip, char* __restrict__ ws) {
  __shared__ __attribute__((aligned(16))) char smem[K2_RED + 24 * 1024];
  STAMP_BEGIN();
  const int tid = threadIdx.x, lane = tid & 63, wid = __builtin_amdgcn_readfirstlane(tid >> 6);
  const int d = (blockIdx.x & 7) * 72 + (blockIdx.x >> 3);
  const u32x4* kx = (const u32x4*)(ws + WS_KX) + (size_t)d * 256;
  const u32x4 kA = kx[tid];
  u32x4 kB = kx[tid > 0 ? tid - 1 : 0];
  const u32x4 xh = ((const u32x4*)(ws + WS_XHT))[(size_t)tid * D + d];
  const float* mp = (const float*)(ws + WS_MODP);
  float md[6];
#pragma unroll
  for (int i = 0; i < 6; ++i) md[i] = bfr(mp[i * D + d] + mp[MODN + i * D + d] + mp[2 * MODN + i * D + d]);
  md[1] = bfr(1.0f + md[1]); md[4] = bfr(1.0f + md[4]);
  const int se = ((const int*)(ws + WS_KSC))[d];
  const float dk = bf2f(dskip[d]);
  if (tid == 0) {
    float* mf = (float*)(ws + WS_MODF);
#pragma unroll
    for (int i = 0; i < 6; ++i) mf[i * D + d] = md[i];
  }
  const float sa = md[0], c1 = md[1], ga = md[2];
  const int he = 14 - __builtin_amdgcn_frexp_expf(24.5f * fabsf(c1) + fabsf(sa));
  if (tid == 0) kB = u32x4{0, 0, 0, 0};
  {
    char* p = smem + (255 - tid) * 16;
    const u32 e0 = __builtin_amdgcn_alignbit(kA[1], kA[0], 16), e1 = __builtin_amdgcn_alignbit(kA[2], kA[1], 16);
    const u32 e2 = __builtin_amdgcn_alignbit(kA[3], kA[2], 16), e3 = __builtin_amdgcn_alignbit(kB[0], kA[3], 16);
    const u32 e4 = __builtin_amdgcn_alignbit(kB[1], kB[0], 16);
    *(u32x4*)(p + 0 * KCP) = u32x4{e1, e2, e3, e4};
    *(u32x4*)(p + 1 * KCP) = u32x4{kA[1], kA[2], kA[3], kB[0]};
    *(u32x4*)(p + 2 * KCP) = u32x4{e0, e1, e2, e3};
    *(u32x4*)(p + 3 * KCP) = kA;
    if (tid < 8) {
      *(u32x4*)(smem + (tid & 3) * KCP + (256 + (tid >> 2)) * 16) = u32x4{0, 0, 0, 0};
    }
    u32x4 hh;
#pragma unroll
    for (int i = 0; i < 4; ++i) {
      const u32 m = pk_bf(bflo(xh[i]) * c1, bfhi(xh[i]) * c1);
      const u32 h = pk_bf(bflo(m) + sa, bfhi(m) + sa);
      hh[i] = pk_h(__builtin_amdgcn_ldexpf(bflo(h), he), __builtin_amdgcn_ldexpf(bfhi(h), he));
    }
    *(u32x4*)(smem + K2_H + 512 + tid * 16) = hh;
    if (tid < 32) *(u32x4*)(smem + K2_H + tid * 16) = u32x4{0, 0, 0, 0};
  }
  __syncthreads();
  const int r = lane & 15, g = lane >> 4;
  f32x4 acc[8];
#pragma unroll
  for (int i = 0; i < 8; ++i) acc[i] = f32x4{0, 0, 0, 0};
  const char* abase = smem + (r & 3) * KCP + 2 * (2044 + 8 - 8 * g - (r & 12));
  const char* bbase = smem + K2_H + 512 + 2 * (16 * r - 8 * g + 8);
#pragma unroll
  for (int cls = 0; cls < 2; ++cls) {
    const int i0 = 2 * wid + cls;
    const char* ap = abase - 64 * (7 - i0);
    const char* bp = bbase + 64 * (i0 - 7);
    f16x8 A[8], B[8];
#pragma unroll
    for (int m = 0; m < 8; ++m) {
      const u32x2 lo = *(const u32x2*)(ap - 512 * m), hi = *(const u32x2*)(ap - 512 * m + 8);
      A[m] = __builtin_bit_cast(f16x8, u32x4{lo[0], lo[1], hi[0], hi[1]});
      B[m] = *(const f16x8*)(bp + 512 * m);
    }
#pragma unroll
    for (int n = 0; n < 8; ++n)
#pragma unroll
      for (int k = 0; k <= n; ++k) acc[n] = __builtin_amdgcn_mfma_f32_16x16x32_f16(A[n - k], B[k], acc[n], 0, 0, 0);
  }
  f32x4* red = (f32x4*)(smem + K2_RED);
#pragma unroll
  for (int n = 0; n < 8; ++n) {
    const int o = n >> 1;
    if (o != wid) red[(n * 3 + (wid > o ? wid - 1 : wid)) * 64 + lane] = acc[n];
  }
  __syncthreads();
#pragma unroll
  for (int e = 0; e < 2; ++e) {
    const int n = 2 * wid + e;
    f32x4 s = wid == 0 ? acc[e] : wid == 1 ? acc[2 + e] : wid == 2 ? acc[4 + e] : acc[6 + e];
#pragma unroll
    for (int j = 0; j < 3; ++j) s += red[(n * 3 + j) * 64 + lane];
    const int t = 256 * n + 16 * r + 4 * g;
    const u32x2 hv = *(const u32x2*)(smem + K2_H + 512 + 2 * t);
    u32x2 o;
#pragma unroll
    for (int j = 0; j < 2; ++j) {
      const u32 hw = hv[j];
      const f16x2 hp = __builtin_bit_cast(f16x2, hw);
      const u32 y = pk_bf(__builtin_amdgcn_ldexpf(s[2 * j], -se - he), __builtin_amdgcn_ldexpf(s[2 * j + 1], -se - he));
      const u32 sk = pk_bf(__builtin_amdgcn_ldexpf((float)hp[0], -he) * dk, __builtin_amdgcn_ldexpf((float)hp[1], -he) * dk);
      const u32 ys = pk_bf(bflo(y) + bflo(sk), bfhi(y) + bfhi(sk));
      o[j] = pk_bf(ga * bflo(ys), ga * bfhi(ys));
    }
    *(u32x2*)(ws + WS_RT + ((size_t)(t >> 3) * D + d) * 16 + (t & 4) * 2) = o;
  }
  STAMP_END(1);
}

__global__ __launch_bounds__(576) void k3_kernel(const u16* __restrict__ x, char* __restrict__ ws) {
  __shared__ float red[2][9][8];
  STAMP_BEGIN();
  const int tid = threadIdx.x, lane = tid & 63, wid = tid >> 6;
  const int tq = (blockIdx.x & 7) * 32 + (blockIdx.x >> 3);
  const int tok = lane & 7, piece = tid >> 3, ks = piece >> 2, g = piece & 3;
  const int dA = 32 * ks + 4 * g, t = 8 * tq + tok;
  const u32x2 xa = *(const u32x2*)(x + (size_t)t * D + dA), xb = *(const u32x2*)(x + (size_t)t * D + dA + 16);
  const u16* rt = (const u16*)(ws + WS_RT) + ((size_t)tq * D + dA) * 8 + tok;
  u16 rv[8];
#pragma unroll
  for (int j = 0; j < 4; ++j) { rv[j] = rt[8 * j]; rv[4 + j] = rt[128 + 8 * j]; }
  const float* mf = (const float*)(ws + WS_MODF);
  const f32x4 sbA = *(const f32x4*)(mf + 3 * D + dA), sbB = *(const f32x4*)(mf + 3 * D + dA + 16);
  const f32x4 cbA = *(const f32x4*)(mf + 4 * D + dA), cbB = *(const f32x4*)(mf + 4 * D + dA + 16);
  u32 p[4];
  p[0] = pk_bf(bflo(xa[0]) + bf2f(rv[0]), bfhi(xa[0]) + bf2f(rv[1]));
  p[1] = pk_bf(bflo(xa[1]) + bf2f(rv[2]), bfhi(xa[1]) + bf2f(rv[3]));
  p[2] = pk_bf(bflo(xb[0]) + bf2f(rv[4]), bfhi(xb[0]) + bf2f(rv[5]));
  p[3] = pk_bf(bflo(xb[1]) + bf2f(rv[6]), bfhi(xb[1]) + bf2f(rv[7]));
  u16* x1 = (u16*)(ws + WS_X1) + (size_t)t * D + dA;
  *(u32x2*)x1 = u32x2{p[0], p[1]};
  *(u32x2*)(x1 + 16) = u32x2{p[2], p[3]};
  float e[8];
#pragma unroll
  for (int j = 0; j < 4; ++j) { e[2 * j] = bflo(p[j]); e[2 * j + 1] = bfhi(p[j]); }
  float s = ((e[0] + e[1]) + (e[2] + e[3])) + ((e[4] + e[5]) + (e[6] + e[7]));
  s = sum_hi3(s);
  if (lane < 8) red[0][wid][lane] = s;
  __syncthreads();
  float mean = 0;
#pragma unroll
  for (int w = 0; w < 9; ++w) mean += red[0][w][tok];
  mean *= (1.0f / D);
  float q = 0;
#pragma unroll
  for (int j = 0; j < 8; ++j) { const float dl = e[j] - mean; q += dl * dl; }
  q = sum_hi3(q);
  if (lane < 8) red[1][wid][lane] = q;
  __syncthreads();
  float var = 0;
#pragma unroll
  for (int w = 0; w < 9; ++w) var += red[1][w][tok];
  var *= (1.0f / D);
  const float m = bfr(mean);
  const float rs = __builtin_amdgcn_rcpf(bfr(__builtin_amdgcn_sqrtf(bfr(bfr(var) + 1e-5f))));
  u32x4 o;
#pragma unroll
  for (int j = 0; j < 4; ++j) {
    const float c0 = j < 2 ? cbA[2 * j] : cbB[2 * j - 4], c1 = j < 2 ? cbA[2 * j + 1] : cbB[2 * j - 3];
    const float s0 = j < 2 ? sbA[2 * j] : sbB[2 * j - 4], s1 = j < 2 ? sbA[2 * j + 1] : sbB[2 * j - 3];
    const u32 dd = pk_bf(e[2 * j] - m, e[2 * j + 1] - m);
    const u32 xh = pk_bf(bflo(dd) * rs, bfhi(dd) * rs);
    const u32 mm = pk_bf(bflo(xh) * c0, bfhi(xh) * c1);
    o[j] = pk_bf(bflo(mm) + s0, bfhi(mm) + s1);
  }
  ((u32x4*)(ws + WS_H2T))[((size_t)(tq >> 1) * 18 + ks) * 64 + 16 * g + 8 * (tq & 1) + tok] = o;
  STAMP_END(2);
}

#define WAIT_V(n) asm volatile("s_waitcnt vmcnt(" #n ")" ::: "memory")
#define WAIT_L0() asm volatile("s_waitcnt lgkmcnt(0)" ::: "memory")
#define BAR() __builtin_amdgcn_s_barrier()
__device__ __forceinline__ void glds16(const void* base, u32 voff, u32 lds) {
  asm volatile("s_mov_b32 m0, %2\n\ts_nop 0\n\tglobal_load_lds_dwordx4 %0, %1" ::"v"(voff), "s"(base), "s"(lds) : "memory");
}
__device__ __forceinline__ void glds16_q(const void* base, u32 voff, u32 lds) {
  asm volatile("s_mov_b32 m0, %2\n\ts_mov_b64 exec, 0xffff\n\tglobal_load_lds_dwordx4 %0, %1\n\ts_mov_b64 exec, -1" ::"v"(voff), "s"(base), "s"(lds) : "memory");
}
__device__ __forceinline__ u32 lds_addr(const void* p) { return (u32)(size_t)(const LAS char*)p; }
__device__ __forceinline__ bf16x8 tr_frag(const char* p, int o2) {
  const s16x4 a = __builtin_amdgcn_ds_read_tr16_b64_v4i16((LAS s16x4*)p);
  const s16x4 b = __builtin_amdgcn_ds_read_tr16_b64_v4i16((LAS s16x4*)(p + o2));
  return __builtin_bit_cast(bf16x8, __builtin_shufflevector(a, b, 0, 1, 2, 3, 4, 5, 6, 7));
}

#define ABL 0
#define GST 2
__device__ __forceinline__ void st16(char* p, u32x4 v) {
  asm volatile("global_store_dwordx4 %0, %1, off sc1\n\ts_nop 1" ::"v"(p), "v"(v) : "memory");
}
constexpr int G1_W1 = 16384, G1_W3 = G1_W1 + 9216 + 16, G1_STAGE = 35072, G1_LDS = 4 * G1_STAGE;

__global__ __launch_bounds__(512, 2) void k4_kernel(const u16* __restrict__ w1, const u16* __restrict__ w3, char* __restrict__ ws) {
  extern __shared__ __attribute__((aligned(16))) char smem[];
  const int tid = threadIdx.x, lane = tid & 63, wid = __builtin_amdgcn_readfirstlane(tid >> 6);
  STAMP_BEGIN();
  const int mt = (blockIdx.x >> 3) & 7, ft = (blockIdx.x & 7) * 4 + (blockIdx.x >> 6);
  const int wm = wid & 3, wn = wid >> 2;
  const char* tsrc = ws + WS_H2T + (size_t)(16 * mt + 2 * wid) * 18 * 1024;
  const char* wsrc = (const char*)(wn ? w3 : w1) + (size_t)(8 * wm) * (F * 2) + ft * 288;
  const int x1 = 64 + lane, x2 = 128 + (lane & 15);
  const u32 o0 = (lane / 18) * (F * 2) + (lane % 18) * 16, o1 = (x1 / 18) * (F * 2) + (x1 % 18) * 16, o2 = (x2 / 18) * (F * 2) + (x2 % 18) * 16;
  const u32 ol = lane * 16;
  const u32 tdst = lds_addr(smem) + 2 * wid * 1024, wdst = lds_addr(smem) + (wn ? G1_W3 : G1_W1) + wm * 2304;
  auto stage = [&](int p) {
    const u32 sb = (p & 3) * G1_STAGE;
    glds16(tsrc + p * 1024, ol, sb + tdst);
    glds16(tsrc + (18 + p) * 1024, ol, sb + tdst + 1024);
    const char* wp = wsrc + (size_t)p * 32 * (F * 2);
    glds16(wp, o0, sb + wdst);
    glds16(wp, o1, sb + wdst + 1024);
    glds16_q(wp, o2, sb + wdst + 2048);
  };
  const int g = lane >> 4, q = (lane >> 2) & 3, pc = lane & 3;
  const int boff = wm * 4096 + lane * 16;
  const int aoff = G1_W1 + (4 * g + q) * 288 + wn * 144 + 8 * pc;
  const int moff = (pc < 2 ? G1_W1 : G1_W3) + (4 * g + q) * 288 + wn * 144 + 128 + 8 * (pc & 1);
  f32x4 acc[9][4];
#pragma unroll
  for (int i = 0; i < 9; ++i)
#pragma unroll
    for (int j = 0; j < 4; ++j) acc[i][j] = f32x4{0, 0, 0, 0};

  stage(0); stage(1); stage(2);
  WAIT_V(10); BAR();
  STAMP_MID(0);
  if (wn) BAR();
#pragma unroll
  for (int p = 0; p < 18; ++p) {
    const char* sb = smem + (p & 3) * G1_STAGE;
    bf16x8 A[9], B[4];
#pragma unroll
    for (int j = 0; j < 4; ++j) B[j] = *(const bf16x8*)(sb + boff + j * 1024);
#pragma unroll
    for (int i = 0; i < 4; ++i) {
      A[i] = tr_frag(sb + aoff + 32 * i, 16 * 288);
      A[4 + i] = tr_frag(sb + aoff + (G1_W3 - G1_W1) + 32 * i, 16 * 288);
    }
    A[8] = tr_frag(sb + moff, 16 * 288);
    if (p + 3 < 18) stage(p + 3);
    if (p < 15) WAIT_V(10); else if (p == 15) WAIT_V(5); else if (p == 16) WAIT_V(0);
    WAIT_L0(); SCHED();
    BAR();
    __builtin_amdgcn_s_setprio(1);
#pragma unroll
    for (int i = 0; i < 9; ++i)
#pragma unroll
      for (int j = 0; j < 4; ++j) acc[i][j] = __builtin_amdgcn_mfma_f32_16x16x32_bf16(A[i], B[j], acc[i][j], 0, 0, 0);
    __builtin_amdgcn_s_setprio(0);
    SCHED();
    if (p < 17 || !wn) BAR();
  }
  STAMP_MID(1);
  const int P = ft >> 1, qq = 2 * (ft & 1) + wn;
  char* gbase = ws + WS_GT + ((size_t)(16 * mt + 4 * wm) * 144 + 9 * P) * 1024;
  auto glu2 = [&](float g0, float g1, float u0, float u1) -> u32 {
    const u32 gg = pk_bf(g0, g1), uu = pk_bf(u0, u1);
    const u32 ss = pk_bf(silu_f(bflo(gg)), silu_f(bfhi(gg)));
    return pk_bf(bflo(ss) * bflo(uu), bfhi(ss) * bfhi(uu));
  };
#pragma unroll
  for (int j = 0; j < 4; ++j)
#pragma unroll
    for (int s = 0; s < 2; ++s) {
      u32x4 o;
#pragma unroll
      for (int h = 0; h < 2; ++h) {
        const f32x4 a = acc[2 * s + h][j], u = acc[4 + 2 * s + h][j];
        o[2 * h] = glu2(a[0], a[1], u[0], u[1]);
        o[2 * h + 1] = glu2(a[2], a[3], u[2], u[3]);
      }
      st16(gbase + (size_t)j * 144 * 1024 + (2 * qq + s) * 1024 + lane * 16, o);
    }
#pragma unroll
  for (int jp = 0; jp < 2; ++jp) {
    float a[4], u[4];
#pragma unroll
    for (int e = 0; e < 4; ++e) {
      const float m0 = acc[8][2 * jp][e], m1 = acc[8][2 * jp + 1][e];
      auto r = __builtin_amdgcn_permlane32_swap(__builtin_bit_cast(u32, m0), __builtin_bit_cast(u32, m1), false, false);
      a[e] = __builtin_bit_cast(float, (u32)r[0]); u[e] = __builtin_bit_cast(float, (u32)r[1]);
    }
    const u32x2 o = {glu2(a[0], a[1], u[0], u[1]), glu2(a[2], a[3], u[2], u[3])};
    const int j = 2 * jp + (lane >> 5);
    *(u32x2*)(gbase + (size_t)j * 144 * 1024 + 8 * 1024 + (16 * (2 * (qq & 1) + (g & 1)) + (lane & 15)) * 16 + 8 * (qq >> 1)) = o;
  }
  STAMP_END(3);
}

constexpr int G2_DEPTH = 4;
constexpr int G2_NS = 18;
constexpr int G2_LDS = 140 * 1024;

template <int TT>
__device__ __forceinline__ void k5_body(const u16* __restrict__ w2, char* __restrict__ ws, u16* __restrict__ out, char* smem, int tile0, int cb) {
  STAMP_BEGIN();
  const int tid = threadIdx.x, lane = tid & 63, wid = __builtin_amdgcn_readfirstlane(tid >> 6);
  const int g = lane >> 4, q = (lane >> 2) & 3, pc = lane & 3;
  char* ring = smem + wid * G2_DEPTH * 4096;
  const char* gsrc = ws + WS_GT + ((size_t)(ABL == 3 || ABL == 5 ? 0 : tile0) * 144 + G2_NS * wid) * 1024;
  const char* wsrc = (const char*)w2 + (size_t)(32 * G2_NS * wid) * (D * 2) + (ABL == 4 || ABL == 5 ? 0 : cb) * 128;
  const u32 ol = lane * 16, ow = (lane >> 3) * (D * 2) + (((lane & 7) ^ (((lane >> 4) & 3) << 1)) * 16);
  const u32 rbase = lds_addr(ring);
  u32x4 B[G2_DEPTH][TT];
  auto stage = [&](int s) {
    const int pp = s / 9, i = s % 9, slot = s % G2_DEPTH;
    const int f0 = 288 * pp + (i < 8 ? 72 * (i >> 1) + 32 * (i & 1) : 64);
#pragma unroll
    for (int j = 0; j < (ABL == 1 ? 0 : 4); ++j) glds16(wsrc + (size_t)(f0 + (i < 8 ? 8 : 72) * j) * (D * 2), ow, rbase + slot * 4096 + j * 1024);
#pragma unroll
    for (int j = 0; j < TT; ++j)
      if (ABL == 2) B[slot][j] = u32x4{ol, ol, ol, ol}; else
      asm volatile("global_load_dwordx4 %0, %1, %2" : "=v"(B[slot][j]) : "v"(ol), "s"(gsrc + ((size_t)j * 144 + s) * 1024) : "memory");
  };
  const int sw = (2 * g + (q >> 1)) & 3;
  int aoff[4];
#pragma unroll
  for (int c = 0; c < 4; ++c) aoff[c] = (4 * g + q) * 128 + (((2 * (c ^ sw)) + (pc >> 1)) * 16) + 8 * (pc & 1);
  f32x4 acc[4][TT];
#pragma unroll
  for (int c = 0; c < 4; ++c)
#pragma unroll
    for (int j = 0; j < TT; ++j) acc[c][j] = f32x4{0, 0, 0, 0};
#pragma unroll
  for (int s = 0; s < G2_DEPTH - 1; ++s) stage(s);
#pragma unroll
  for (int s = 0; s < G2_NS; ++s) {
    constexpr int AH = G2_DEPTH - 1;
    if (s + AH < G2_NS) stage(s + AH);
    const int left = (s + AH < G2_NS ? AH : G2_NS - 1 - s) * (ABL == 1 ? TT : ABL == 2 ? 4 : TT + 4), slot = s % G2_DEPTH;
    if (TT == 5)
      asm volatile("s_waitcnt vmcnt(%5)" : "+v"(B[slot][0]), "+v"(B[slot][1]), "+v"(B[slot][2]), "+v"(B[slot][3]), "+v"(B[slot][TT - 1]) : "i"(left) : "memory");
    else
      asm volatile("s_waitcnt vmcnt(%4)" : "+v"(B[slot][0]), "+v"(B[slot][1]), "+v"(B[slot][2]), "+v"(B[slot][3]) : "i"(left) : "memory");
    if (s == 0) STAMP_MID(0);
    if (s == 9) STAMP_MID(1);
    const char* sb = ring + slot * 4096;
    bf16x8 A[4];
#pragma unroll
    for (int c = 0; c < 4; ++c) if (ABL != 6) A[c] = tr_frag(sb + aoff[c], 2048);
    if (ABL == 7) asm volatile("" ::"v"(A[0]), "v"(A[1]), "v"(A[2]), "v"(A[3]));
#pragma unroll
    for (int c = 0; c < 4; ++c)
#pragma unroll
      for (int j = 0; j < TT; ++j) if (ABL < 6)
        acc[c][j] = __builtin_amdgcn_mfma_f32_16x16x32_bf16(A[c], __builtin_bit_cast(bf16x8, B[slot][j]), acc[c][j], 0, 0, 0);
    SCHED();
  }
  STAMP_MID(2);
  const int oc = wid & 3, oh = wid >> 2;
  const int col = 64 * cb + 16 * oc + 4 * g;
  constexpr int NJ = (TT + 1) / 2;
  const f32x4 gb = *(const f32x4*)((const float*)(ws + WS_MODF) + 5 * D + col);
  u32x2 xr[NJ];
#pragma unroll
  for (int jj = 0; jj < NJ; ++jj) {
    const int j = min(2 * jj + oh, TT - 1);
    xr[jj] = *(const u32x2*)((const u16*)(ws + WS_X1) + (size_t)(16 * (tile0 + j) + (lane & 15)) * D + col);
  }
  __syncthreads();
  STAMP_MID(3);
  f32x4* red = (f32x4*)smem;
#pragma unroll
  for (int c = 0; c < 4; ++c)
#pragma unroll
    for (int j = 0; j < TT; ++j) {
      const int owner = c + 4 * (j & 1);
      if (owner != wid) red[((c * TT + j) * 7 + (wid > owner ? wid - 1 : wid)) * 64 + lane] = acc[c][j];
    }
  __syncthreads();
#pragma unroll
  for (int jj = 0; jj < NJ; ++jj) {
    const int j = 2 * jj + oh;
    if (j < TT) {
      constexpr int j0 = 0;
      f32x4 s;
      {
        const int ja = 2 * jj, jb = 2 * jj + 1 < TT ? 2 * jj + 1 : 2 * jj;
        const f32x4 s0 = oh ? acc[0][jb] : acc[0][ja], s1 = oh ? acc[1][jb] : acc[1][ja], s2 = oh ? acc[2][jb] : acc[2][ja], s3 = oh ? acc[3][jb] : acc[3][ja];
        s = oc == 0 ? s0 : oc == 1 ? s1 : oc == 2 ? s2 : s3;
      }
      (void)j0;
#pragma unroll
      for (int k = 0; k < 7; ++k) s += red[((oc * TT + j) * 7 + k) * 64 + lane];
      u32x2 o;
#pragma unroll
      for (int h = 0; h < 2; ++h) {
        const u32 y = pk_bf(s[2 * h], s[2 * h + 1]);
        const u32 z = pk_bf(gb[2 * h] * bflo(y), gb[2 * h + 1] * bfhi(y));
        o[h] = pk_bf(bflo(xr[jj][h]) + bflo(z), bfhi(xr[jj][h]) + bfhi(z));
      }
      *(u32x2*)(out + (size_t)(16 * (tile0 + j) + (lane & 15)) * D + col) = o;
    }
  }
  STAMP_END(4);
}

__global__ __launch_bounds__(512, 2) void k5_kernel(const u16* __restrict__ w2, char* __restrict__ ws, u16* __restrict__ out) {
  extern __shared__ __attribute__((aligned(16))) char smem[];
  const int xcd = blockIdx.x & 7, off = blockIdx.x >> 3;
  const int id = (xcd < 4 ? xcd * 32 : 128 + (xcd - 4) * 31) + off;
  const int rb = id / 9, cb = id % 9;
  if (rb < 16) k5_body<5>(w2, ws, out, smem, 5 * rb, cb);
  else k5_body<4>(w2, ws, out, smem, 80 + 4 * (rb - 16), cb);
}

extern "C" void kernel_launch(void* const* d_in, const int* in_sizes, int n_in, void* d_out, int out_size, void* d_ws, size_t ws_size,
                              hipStream_t stream) {
  const u16* u = (const u16*)d_in[0];
  char* ws = (char*)d_ws;
  (void)hipFuncSetAttribute((const void*)k4_kernel, hipFuncAttributeMaxDynamicSharedMemorySize, G1_LDS);
  (void)hipFuncSetAttribute((const void*)k5_kernel, hipFuncAttributeMaxDynamicSharedMemorySize, G2_LDS);
  k1_kernel<<<K1_MOD + K1_LN + K1_KG, 256, 0, stream>>>(u, (const u16*)d_in[1], (const u16*)d_in[2], (const u16*)d_in[3], (const u16*)d_in[4],
                                                        (const u16*)d_in[5], (const u16*)d_in[6], (const u16*)d_in[7], (const u16*)d_in[13],
                                                        (const u16*)d_in[14], ws);
  k2_kernel<<<D, 256, 0, stream>>>((const u16*)d_in[8], ws);
  k3_kernel<<<256, 576, 0, stream>>>(u, ws);
  k4_kernel<<<256, 512, G1_LDS, stream>>>((const u16*)d_in[10], (const u16*)d_in[11], ws);
  k5_kernel<<<252, 512, G2_LDS, stream>>>((const u16*)d_in[12], ws, (u16*)d_out);
}
```

```cpp
#include <hip/hip_runtime.h>
#include <hip/hip_bf16.h>

typedef unsigned short u16;
typedef unsigned int u32;
typedef __attribute__((ext_vector_type(2))) float f32x2;
typedef __attribute__((ext_vector_type(4))) float f32x4;
typedef __attribute__((ext_vector_type(2))) u32 u32x2;
typedef __attribute__((ext_vector_type(4))) u32 u32x4;
typedef __attribute__((ext_vector_type(4))) short s16x4;
typedef __attribute__((ext_vector_type(8))) short s16x8;
typedef __attribute__((ext_vector_type(2))) __bf16 bf16x2;
typedef __attribute__((ext_vector_type(8))) __bf16 bf16x8;
typedef __attribute__((ext_vector_type(2))) _Float16 f16x2;
typedef __attribute__((ext_vector_type(8))) _Float16 f16x8;
#define LAS __attribute__((address_space(3)))
#define GAS __attribute__((address_space(1)))

constexpr int L = 2048, D = 576, NM = 128, F = 4608, MODN = 6 * D;
constexpr int KSPLIT = 3;

constexpr size_t WS_MODP = 0;
constexpr size_t WS_MODF = 65536;
constexpr size_t WS_XHT  = 131072;
constexpr size_t ACT_B   = (size_t)L * D * 2;
constexpr size_t WS_KX   = WS_XHT + ACT_B;
constexpr size_t WS_RT   = WS_KX + ACT_B;
constexpr size_t WS_X1   = WS_RT + ACT_B;
constexpr size_t WS_H2T  = WS_X1 + ACT_B;
constexpr size_t WS_GT   = WS_H2T + ACT_B;
constexpr size_t WS_XCH  = WS_GT + (size_t)L * F * 2;
constexpr size_t WS_FLAG = 81920;

#define WAIT_V(n) asm volatile("s_waitcnt vmcnt(" #n ")" ::: "memory")
#define WAIT_L0() asm volatile("s_waitcnt lgkmcnt(0)" ::: "memory")
#define BAR() __builtin_amdgcn_s_barrier()
#define SCHED() __builtin_amdgcn_sched_barrier(0)
#define STAMP_BEGIN()
#define STAMP_MID(i)
#define STAMP_END(k)
__device__ __forceinline__ float bf2f(u16 v) { return __builtin_bit_cast(float, (u32)v << 16); }
__device__ __forceinline__ float bflo(u32 v) { return __builtin_bit_cast(float, v << 16); }
__device__ __forceinline__ float bfhi(u32 v) { return __builtin_bit_cast(float, v & 0xffff0000u); }
__device__ __forceinline__ u32 pk_bf(float a, float b) { f32x2 v = {a, b}; return __builtin_bit_cast(u32, __builtin_convertvector(v, bf16x2)); }
__device__ __forceinline__ u32 pk_h(float a, float b) { f32x2 v = {a, b}; return __builtin_bit_cast(u32, __builtin_convertvector(v, f16x2)); }
__device__ __forceinline__ float bfr(float a) { return bflo(pk_bf(a, a)); }

template <int CTRL> __device__ __forceinline__ float dpp(float v) {
  return __builtin_bit_cast(float, __builtin_amdgcn_update_dpp(0, __builtin_bit_cast(int, v), CTRL, 0xf, 0xf, true));
}
__device__ __forceinline__ float sum16x(float v) {
  u32 u = __builtin_bit_cast(u32, v);
  auto r = __builtin_amdgcn_permlane16_swap(u, u, false, false);
  return __builtin_bit_cast(float, (u32)r[0]) + __builtin_bit_cast(float, (u32)r[1]);
}
__device__ __forceinline__ float sum32x(float v) {
  u32 u = __builtin_bit_cast(u32, v);
  auto r = __builtin_amdgcn_permlane32_swap(u, u, false, false);
  return __builtin_bit_cast(float, (u32)r[0]) + __builtin_bit_cast(float, (u32)r[1]);
}
__device__ __forceinline__ float wave_sum(float v) {
  v += dpp<0xB1>(v);
  v += dpp<0x4E>(v);
  v += dpp<0x141>(v);
  v += dpp<0x140>(v);
  return sum32x(sum16x(v));
}
__device__ __forceinline__ float sum_hi3(float v) {
  v += dpp<0x128>(v);
  return sum32x(sum16x(v));
}
__device__ __forceinline__ float silu_f(float g) { return g * __builtin_amdgcn_rcpf(1.0f + __builtin_amdgcn_exp2f(-1.4426950408889634f * g)); }

__device__ __forceinline__ u32 touch(const void* p) {
  u32 r;
  asm volatile("global_load_dword %0, %1, off" : "=v"(r) : "v"(p) : "memory");
  return r;
}
#define TOUCH_DONE(r) asm volatile("s_waitcnt vmcnt(0)" : "+v"(r)::"memory")

constexpr int K1_MOD = 54 * KSPLIT, K1_LN = 256, K1_KG = D;
constexpr int TROW = 528;

typedef f32x2 cplx;
__device__ __forceinline__ cplx cmul(cplx a, cplx z) {
  cplx t, r;
  asm("v_pk_mul_f32 %0, %1, %2 op_sel_hi:[1,0]" : "=v"(t) : "v"(a), "v"(z));
  asm("v_pk_fma_f32 %0, %1, %2, %3 op_sel:[1,1,0] op_sel_hi:[0,1,1] neg_lo:[1,0,0]" : "=v"(r) : "v"(a), "v"(z), "v"(t));
  return r;
}
__device__ __forceinline__ cplx cexpl(float al2, float brev, float l) {
  const float m = __builtin_amdgcn_exp2f(al2 * l);
  const float ph = __builtin_amdgcn_fractf(brev * l);
  return cplx{m * __builtin_amdgcn_cosf(ph), m * __builtin_amdgcn_sinf(ph)};
}

extern "C" __global__ __launch_bounds__(256) void k1_kernel(const u16* __restrict__ x, const u16* __restrict__ Are, const u16* __restrict__ Aim,
                                                 const u16* __restrict__ Bre, const u16* __restrict__ Bim, const u16* __restrict__ Cre,
                                                 const u16* __restrict__ Cim, const u16* __restrict__ logdt, const u16* __restrict__ cvec,
                                                 const u16* __restrict__ wmod, char* __restrict__ ws) {
  __shared__ __attribute__((aligned(16))) char smem[64 * TROW + 2624];
  STAMP_BEGIN();
  const int tid = threadIdx.x, lane = tid & 63, wid = tid >> 6;
  int b = blockIdx.x;
  u32 pf = 0;
  if (b < 16) {
    unsigned long long pc;
    u32 len;
    asm volatile("s_getpc_b64 %0\n.Lk1_here:\n\ts_mov_b32 %1, kz_end-.Lk1_here" : "=s"(pc), "=s"(len));
    const u32 o = ((b >> 3) * 256 + tid) * 128;
    if (o < len) pf = touch((const void*)(pc + o));
  }
#define K1_DONE() TOUCH_DONE(pf)
  b = b < K1_KG ? b + K1_MOD + K1_LN : b - K1_KG;
  if (b < K1_MOD) {
    const int cb = b % 54, kq = b / 54, cc = tid & 7, rg = tid >> 3;
    u32x4 w[6]; u16 cv[6];
#pragma unroll
    for (int i = 0; i < 6; ++i) {
      const int row = 192 * kq + rg + 32 * i;
      w[i] = *(const u32x4*)(wmod + (size_t)row * MODN + 64 * cb + 8 * cc);
      cv[i] = cvec[row];
    }
    float acc[8] = {0, 0, 0, 0, 0, 0, 0, 0};
#pragma unroll
    for (int i = 0; i < 6; ++i) {
      const float s = bfr(silu_f(bf2f(cv[i])));
#pragma unroll
      for (int j = 0; j < 4; ++j) { acc[2 * j] += s * bflo(w[i][j]); acc[2 * j + 1] += s * bfhi(w[i][j]); }
    }
#pragma unroll
    for (int j = 0; j < 8; ++j) acc[j] = sum_hi3(acc[j]);
    float* red = (float*)smem;
    if (lane < 8) {
      *(f32x4*)(red + wid * 64 + lane * 8) = f32x4{acc[0], acc[1], acc[2], acc[3]};
      *(f32x4*)(red + wid * 64 + lane * 8 + 4) = f32x4{acc[4], acc[5], acc[6], acc[7]};
    }
    __syncthreads();
    if (tid < 64) ((float*)(ws + WS_MODP))[((cb % 9) * 64 + tid) * 24 + (cb / 9) * 4 + kq] = (red[tid] + red[64 + tid]) + (red[128 + tid] + red[192 + tid]);
    STAMP_END(0);
    return;
  }
  b -= K1_MOD;
  if (b < K1_LN) {
    u32 v[2][4]; u16 t[2];
#pragma unroll
    for (int r = 0; r < 2; ++r) {
      const u16* row = x + (size_t)(8 * b + 2 * wid + r) * D;
#pragma unroll
      for (int i = 0; i < 4; ++i) v[r][i] = *(const u32*)(row + 2 * (lane + 64 * i));
      t[r] = row[512 + lane];
    }
    u32 o[2][5];
#pragma unroll
    for (int r = 0; r < 2; ++r) {
      float e[9];
#pragma unroll
      for (int i = 0; i < 4; ++i) { e[2 * i] = bflo(v[r][i]); e[2 * i + 1] = bfhi(v[r][i]); }
      e[8] = bf2f(t[r]);
      float s = 0;
#pragma unroll
      for (int i = 0; i < 9; ++i) s += e[i];
      const float mean = wave_sum(s) * (1.0f / D);
      float q = 0;
#pragma unroll
      for (int i = 0; i < 9; ++i) { const float dlt = e[i] - mean; q += dlt * dlt; }
      const float var = wave_sum(q) * (1.0f / D);
      const float m = bfr(mean);
      const float rs = __builtin_amdgcn_rcpf(bfr(__builtin_amdgcn_sqrtf(bfr(bfr(var) + 1e-5f))));
#pragma unroll
      for (int i = 0; i < 4; ++i) {
        const u32 dd = pk_bf(e[2 * i] - m, e[2 * i + 1] - m);
        o[r][i] = pk_bf(bflo(dd) * rs, bfhi(dd) * rs);
      }
      o[r][4] = pk_bf(bfr(e[8] - m) * rs, 0.f);
    }
    u32* tile = (u32*)smem;
#pragma unroll
    for (int i = 0; i < 4; ++i) {
      tile[(2 * (lane + 64 * i)) * 4 + wid] = __builtin_amdgcn_perm(o[1][i], o[0][i], 0x05040100);
      tile[(2 * (lane + 64 * i) + 1) * 4 + wid] = __builtin_amdgcn_perm(o[1][i], o[0][i], 0x07060302);
    }
    tile[(512 + lane) * 4 + wid] = __builtin_amdgcn_perm(o[1][4], o[0][4], 0x05040100);
    __syncthreads();
    u32x4* dst = (u32x4*)(ws + WS_XHT) + (size_t)b * D;
#pragma unroll
    for (int i = 0; i < 3; ++i) {
      const int c = tid + 256 * i;
      if (c < D) dst[c] = *(const u32x4*)(tile + c * 4);
    }
    STAMP_END(0);
    return;
  }
  b -= K1_LN;
  {
    const int n = tid & 127, half = __builtin_amdgcn_readfirstlane(tid >> 7);
    const int idx = b * NM + n;
    const float are = bf2f(Are[idx]), aim = bf2f(Aim[idx]), bre = bf2f(Bre[idx]), bim = bf2f(Bim[idx]);
    const float cre = bf2f(Cre[idx]), cim = bf2f(Cim[idx]);
    const float dt = bfr(__builtin_amdgcn_exp2f(1.4426950408889634f * bf2f(logdt[b])));
    const float al2 = (dt * are) * 1.4426950408889634f, brev = (dt * aim) * 0.15915494309189535f;
    cplx c = cmul(cplx{cre, cim}, cplx{dt * bre, dt * bim});
    const int he = (__builtin_amdgcn_frexp_expf(fmaxf(fabsf(c[0]), fabsf(c[1])))) >> 1;
    c = cplx{__builtin_amdgcn_ldexpf(c[0], -he), __builtin_amdgcn_ldexpf(c[1], -he)};
    char* Ut = smem + n * 4;
#define PUT(row, z) (*(u32*)(Ut + (row) * TROW) = pk_h((z)[0], (z)[1]))
    const cplx z128 = cexpl(al2, brev, 128.f);
    if (half == 0) {
      const cplx z8c = cexpl(al2, -brev, 8.f);
      cplx v = {__builtin_amdgcn_ldexpf(1.0f, he), 0.f};
      cplx u = cmul(c, cexpl(al2, brev, 1024.f));
#pragma unroll
      for (int j = 0; j < 16; ++j) {
        PUT(48 + j, v); if (j < 15) v = cmul(v, z8c);
        if (j < 9) { PUT(32 + j, u); if (j < 8) u = cmul(u, z128); }
      }
      const cplx bd = {fabsf(c[0]) + fabsf(c[1]), 0.f};
      PUT(41, bd);
    } else {
      const cplx z1 = cexpl(al2, brev, 1.f);
      cplx u[8];
      u[0] = c;
#pragma unroll
      for (int j = 1; j < 8; ++j) u[j] = cmul(u[j - 1], z1);
#pragma unroll
      for (int j = 0; j < 8; ++j) { PUT(j, u[j]); u[j] = cmul(u[j], z128); PUT(8 + j, u[j]); }
#pragma unroll
      for (int s = 0; s < 2; ++s)
#pragma unroll
        for (int j = 0; j < 4; ++j) { u[2 * j] = cmul(u[2 * j], z128); PUT(16 + 4 * s + j, u[2 * j]); }
#pragma unroll
      for (int s = 0; s < 4; ++s)
#pragma unroll
        for (int j = 0; j < 2; ++j) { u[4 * j] = cmul(u[4 * j], z128); PUT(24 + 2 * s + j, u[4 * j]); }
    }
#undef PUT
    WAIT_L0(); BAR();
    float* Kc = (float*)(smem + 64 * TROW);
    const int wu = __builtin_amdgcn_readfirstlane(wid);
    if (wu < 3) {
      const int r = lane & 15, g = lane >> 4;
      f32x4 acc = {0, 0, 0, 0};
#pragma unroll
      for (int ks = 0; ks < 8; ++ks) {
        const f16x8 a = *(const f16x8*)(smem + (16 * wu + r) * TROW + ks * 64 + g * 16);
        const f16x8 v = *(const f16x8*)(smem + (48 + r) * TROW + ks * 64 + g * 16);
        acc = __builtin_amdgcn_mfma_f32_16x16x32_f16(a, v, acc, 0, 0, 0);
      }
      int o0, sA = 1, sB;
      if (wu == 0) { o0 = (g < 2 ? 4 * g : 120 + 4 * g) + 8 * r; sB = 2; }
      else if (wu == 1) { o0 = g < 2 ? 256 + 64 * g + 4 * r : 256 + 64 * g + 2 * r; sB = g < 2 ? 2 : 32; }
      else { o0 = g < 2 ? 512 + 64 * g + r : (lane == 32 ? 640 : 648); sA = g < 2 ? 16 : 1; sB = g < 2 ? 32 : 4; }
      Kc[o0] = acc[0]; Kc[o0 + sA] = acc[1]; Kc[o0 + sB] = acc[2]; Kc[o0 + sA + sB] = acc[3];
    }
    WAIT_L0(); BAR();
    {
      const int q = tid;
      float k[8];
      if (q < 32) {
#pragma unroll
        for (int e = 0; e < 8; ++e) k[e] = Kc[8 * q + e];
      } else if (q < 64) {
        const float* p = Kc + 256 + 4 * (q - 32);
        k[0] = k[1] = p[0]; k[2] = p[1]; k[3] = k[4] = k[5] = p[2]; k[6] = p[3]; k[7] = p[4];
      } else if (q < 128) {
        const float* p = Kc + 384 + 2 * (q - 64);
        k[0] = k[1] = k[2] = p[0]; k[3] = k[4] = k[5] = p[1]; k[6] = k[7] = p[2];
      } else {
        const float* p = Kc + 512 + (q - 128);
        k[0] = k[1] = k[2] = k[3] = p[0]; k[5] = k[6] = k[7] = p[1]; k[4] = (q & 1) ? p[1] : p[0];
      }
      const int se = 14 - __builtin_amdgcn_frexp_expf(Kc[641]);
      u32x4 o;
#pragma unroll
      for (int e = 0; e < 4; ++e) o[e] = pk_h(__builtin_amdgcn_ldexpf(k[7 - 2 * e], se), __builtin_amdgcn_ldexpf(k[6 - 2 * e], se));
      ((u32x4*)(ws + WS_KX))[(size_t)b * 256 + q] = o;
      if (tid == 0) ((int*)(ws + WS_MODP))[b * 24 + 3] = se;
    }
    K1_DONE();
    STAMP_END(0);
  }
}

#define PF_ROWS 288
constexpr int KCP = 4160;
constexpr int K2_H = 4 * KCP;
constexpr int K2_RED = K2_H + 4608;

extern "C" __global__ __launch_bounds__(256) void k2_kernel(const u16* __restrict__ dskip, const u16* __restrict__ w1, const u16* __restrict__ w3,
                                                            char* __restrict__ ws) {
  __shared__ __attribute__((aligned(16))) char smem[K2_RED + 24 * 1024];
  STAMP_BEGIN();
  const int tid = threadIdx.x, lane = tid & 63, wid = __builtin_amdgcn_readfirstlane(tid >> 6);
  const int d = (blockIdx.x & 7) * 72 + (blockIdx.x >> 3);
  const u32x4* kx = (const u32x4*)(ws + WS_KX) + (size_t)d * 256;
  const u32x4 xh = ((const u32x4*)(ws + WS_XHT))[(size_t)tid * D + d];
  const u32x4 kA = kx[tid];
  u32x2 kB = *(const u32x2*)(kx + max(tid - 1, 0));
  const f32x4* mp = (const f32x4*)(ws + WS_MODP) + d * 6;
  float md[6];
  int se = 0;
#pragma unroll
  for (int i = 0; i < 6; ++i) {
    const f32x4 v = mp[i];
    md[i] = bfr(v[0] + v[1] + v[2]);
    if (i == 0) { const float e = v[3]; se = __builtin_bit_cast(int, e); }
  }
  md[1] = bfr(1.0f + md[1]); md[4] = bfr(1.0f + md[4]);
  const u32 dkw = ((const u32*)dskip)[d >> 1];
  const float dk = (d & 1) ? bfhi(dkw) : bflo(dkw);
  const float sa = md[0], c1 = md[1], ga = md[2];
  const int he = 14 - __builtin_amdgcn_frexp_expf(24.5f * fabsf(c1) + fabsf(sa));
  if (tid == 0) kB = u32x2{0, 0};
  {
    char* p = smem + (255 - tid) * 16;
    const u32 e0 = __builtin_amdgcn_alignbit(kA[1], kA[0], 16), e1 = __builtin_amdgcn_alignbit(kA[2], kA[1], 16);
    const u32 e2 = __builtin_amdgcn_alignbit(kA[3], kA[2], 16), e3 = __builtin_amdgcn_alignbit(kB[0], kA[3], 16);
    const u32 e4 = __builtin_amdgcn_alignbit(kB[1], kB[0], 16);
    *(u32x4*)(p + 0 * KCP) = u32x4{e1, e2, e3, e4};
    *(u32x4*)(p + 1 * KCP) = u32x4{kA[1], kA[2], kA[3], kB[0]};
    *(u32x4*)(p + 2 * KCP) = u32x4{e0, e1, e2, e3};
    *(u32x4*)(p + 3 * KCP) = kA;
    if (tid < 8) {
      *(u32x4*)(smem + (tid & 3) * KCP + (256 + (tid >> 2)) * 16) = u32x4{0, 0, 0, 0};
    }
    u32x4 hh;
#pragma unroll
    for (int i = 0; i < 4; ++i) {
      const u32 m = pk_bf(bflo(xh[i]) * c1, bfhi(xh[i]) * c1);
      const u32 h = pk_bf(bflo(m) + sa, bfhi(m) + sa);
      hh[i] = pk_h(__builtin_amdgcn_ldexpf(bflo(h), he), __builtin_amdgcn_ldexpf(bfhi(h), he));
    }
    *(u32x4*)(smem + K2_H + 512 + tid * 16) = hh;
    if (tid < 32) *(u32x4*)(smem + K2_H + tid * 16) = u32x4{0, 0, 0, 0};
  }
  u32 pf = 0;
  {
    const int i = blockIdx.x * 256 + tid;
    if (i < 2 * PF_ROWS * 72) pf = touch((const char*)(i < PF_ROWS * 72 ? w1 : w3) + (size_t)(i < PF_ROWS * 72 ? i : i - PF_ROWS * 72) * 128);
  }
  STAMP_MID(0);
  WAIT_L0(); BAR();
  STAMP_MID(1);
  if (tid == 0) {
    float* mf = (float*)(ws + WS_MODF);
#pragma unroll
    for (int i = 3; i < 6; ++i) mf[i * D + d] = md[i];
  }
  const int r = lane & 15, g = lane >> 4;
  f32x4 acc[8];
#pragma unroll
  for (int i = 0; i < 8; ++i) acc[i] = f32x4{0, 0, 0, 0};
  const char* abase = smem + (r & 3) * KCP + 2 * (2044 + 8 - 8 * g - (r & 12));
  const char* bbase = smem + K2_H + 512 + 2 * (16 * r - 8 * g + 8);
#pragma unroll
  for (int cls = 0; cls < 2; ++cls) {
    const int i0 = 2 * wid + cls;
    const char* ap = abase - 64 * (7 - i0);
    const char* bp = bbase + 64 * (i0 - 7);
    f16x8 A[8], B[8];
#pragma unroll
    for (int m = 0; m < 8; ++m) {
      const u32x2 lo = *(const volatile LAS u32x2*)(ap - 512 * m), hi = *(const volatile LAS u32x2*)(ap - 512 * m + 8);
      A[m] = __builtin_bit_cast(f16x8, u32x4{lo[0], lo[1], hi[0], hi[1]});
      B[m] = *(const f16x8*)(bp + 512 * m);
    }
#pragma unroll
    for (int n = 0; n < 8; ++n)
#pragma unroll
      for (int k = 0; k <= n; ++k) acc[n] = __builtin_amdgcn_mfma_f32_16x16x32_f16(A[n - k], B[k], acc[n], 0, 0, 0);
  }
  STAMP_MID(2);
  f32x4* red = (f32x4*)(smem + K2_RED);
#pragma unroll
  for (int n = 0; n < 8; ++n) {
    const int o = n >> 1;
    if (o != wid) red[(n * 3 + (wid > o ? wid - 1 : wid)) * 64 + lane] = acc[n];
  }
  WAIT_L0(); BAR();
  STAMP_MID(3);
#pragma unroll
  for (int e = 0; e < 2; ++e) {
    const int n = 2 * wid + e;
    f32x4 s = wid == 0 ? acc[e] : wid == 1 ? acc[2 + e] : wid == 2 ? acc[4 + e] : acc[6 + e];
#pragma unroll
    for (int j = 0; j < 3; ++j) s += red[(n * 3 + j) * 64 + lane];
    const int t = 256 * n + 16 * r + 4 * g;
    const u32x2 hv = *(const u32x2*)(smem + K2_H + 512 + 2 * t);
    u32x2 o;
#pragma unroll
    for (int j = 0; j < 2; ++j) {
      const u32 hw = hv[j];
      const f16x2 hp = __builtin_bit_cast(f16x2, hw);
      const u32 y = pk_bf(__builtin_amdgcn_ldexpf(s[2 * j], -se - he), __builtin_amdgcn_ldexpf(s[2 * j + 1], -se - he));
      const u32 sk = pk_bf(__builtin_amdgcn_ldexpf((float)hp[0], -he) * dk, __builtin_amdgcn_ldexpf((float)hp[1], -he) * dk);
      const u32 ys = pk_bf(bflo(y) + bflo(sk), bfhi(y) + bfhi(sk));
      o[j] = pk_bf(ga * bflo(ys), ga * bfhi(ys));
    }
    *(u32x2*)(ws + WS_RT + ((size_t)(t >> 3) * D + d) * 16 + (t & 4) * 2) = o;
  }
  TOUCH_DONE(pf);
  STAMP_END(1);
}

extern "C" __global__ __launch_bounds__(576) void k3_kernel(const u16* __restrict__ x, char* __restrict__ ws) {
  __shared__ float red[2][9][8];
  STAMP_BEGIN();
  const int tid = threadIdx.x, lane = tid & 63, wid = tid >> 6;
  const int tq = (blockIdx.x & 7) * 32 + (blockIdx.x >> 3);
  if (tid < 36) ((u32*)(ws + WS_FLAG))[blockIdx.x * 36 + tid] = 0;
  const int tok = lane & 7, piece = tid >> 3, ks = piece >> 2, g = piece & 3;
  const int dA = 32 * ks + 4 * g, t = 8 * tq + tok;
  const u32x2 xa = *(const u32x2*)(x + (size_t)t * D + dA), xb = *(const u32x2*)(x + (size_t)t * D + dA + 16);
  const u16* rt = (const u16*)(ws + WS_RT) + ((size_t)tq * D + dA) * 8 + tok;
  u16 rv[8];
#pragma unroll
  for (int j = 0; j < 4; ++j) { rv[j] = rt[8 * j]; rv[4 + j] = rt[128 + 8 * j]; }
  const float* mf = (const float*)(ws + WS_MODF);
  const f32x4 sbA = *(const f32x4*)(mf + 3 * D + dA), sbB = *(const f32x4*)(mf + 3 * D + dA + 16);
  const f32x4 cbA = *(const f32x4*)(mf + 4 * D + dA), cbB = *(const f32x4*)(mf + 4 * D + dA + 16);
  u32 p[4];
  p[0] = pk_bf(bflo(xa[0]) + bf2f(rv[0]), bfhi(xa[0]) + bf2f(rv[1]));
  p[1] = pk_bf(bflo(xa[1]) + bf2f(rv[2]), bfhi(xa[1]) + bf2f(rv[3]));
  p[2] = pk_bf(bflo(xb[0]) + bf2f(rv[4]), bfhi(xb[0]) + bf2f(rv[5]));
  p[3] = pk_bf(bflo(xb[1]) + bf2f(rv[6]), bfhi(xb[1]) + bf2f(rv[7]));
  u16* x1 = (u16*)(ws + WS_X1) + (size_t)t * D + dA;
  *(u32x2*)x1 = u32x2{p[0], p[1]};
  *(u32x2*)(x1 + 16) = u32x2{p[2], p[3]};
  float e[8];
#pragma unroll
  for (int j = 0; j < 4; ++j) { e[2 * j] = bflo(p[j]); e[2 * j + 1] = bfhi(p[j]); }
  float s = ((e[0] + e[1]) + (e[2] + e[3])) + ((e[4] + e[5]) + (e[6] + e[7]));
  s = sum_hi3(s);
  if (lane < 8) red[0][wid][lane] = s;
  __syncthreads();
  float mean = 0;
#pragma unroll
  for (int w = 0; w < 9; ++w) mean += red[0][w][tok];
  mean *= (1.0f / D);
  float q = 0;
#pragma unroll
  for (int j = 0; j < 8; ++j) { const float dl = e[j] - mean; q += dl * dl; }
  q = sum_hi3(q);
  if (lane < 8) red[1][wid][lane] = q;
  __syncthreads();
  float var = 0;
#pragma unroll
  for (int w = 0; w < 9; ++w) var += red[1][w][tok];
  var *= (1.0f / D);
  const float m = bfr(mean);
  const float rs = __builtin_amdgcn_rcpf(bfr(__builtin_amdgcn_sqrtf(bfr(bfr(var) + 1e-5f))));
  u32x4 o;
#pragma unroll
  for (int j = 0; j < 4; ++j) {
    const float c0 = j < 2 ? cbA[2 * j] : cbB[2 * j - 4], c1 = j < 2 ? cbA[2 * j + 1] : cbB[2 * j - 3];
    const float s0 = j < 2 ? sbA[2 * j] : sbB[2 * j - 4], s1 = j < 2 ? sbA[2 * j + 1] : sbB[2 * j - 3];
    const u32 dd = pk_bf(e[2 * j] - m, e[2 * j + 1] - m);
    const u32 xh = pk_bf(bflo(dd) * rs, bfhi(dd) * rs);
    const u32 mm = pk_bf(bflo(xh) * c0, bfhi(xh) * c1);
    o[j] = pk_bf(bflo(mm) + s0, bfhi(mm) + s1);
  }
  ((u32x4*)(ws + WS_H2T))[((size_t)(tq >> 1) * 18 + ks) * 64 + 16 * g + 8 * (tq & 1) + tok] = o;
  STAMP_END(2);
}

__device__ __forceinline__ void glds16(const void* base, u32 voff, u32 lds) {
  asm volatile("s_mov_b32 m0, %2\n\ts_nop 0\n\tglobal_load_lds_dwordx4 %0, %1" ::"v"(voff), "s"(base), "s"(lds) : "memory");
}
__device__ __forceinline__ void glds16_q(const void* base, u32 voff, u32 lds) {
  asm volatile("s_mov_b32 m0, %2\n\ts_mov_b64 exec, 0xffff\n\tglobal_load_lds_dwordx4 %0, %1\n\ts_mov_b64 exec, -1" ::"v"(voff), "s"(base), "s"(lds) : "memory");
}
__device__ __forceinline__ u32 lds_addr(const void* p) { return (u32)(size_t)(const LAS char*)p; }
__device__ __forceinline__ bf16x8 tr_frag(const char* p, int o2) {
  const s16x4 a = __builtin_amdgcn_ds_read_tr16_b64_v4i16((LAS s16x4*)p);
  const s16x4 b = __builtin_amdgcn_ds_read_tr16_b64_v4i16((LAS s16x4*)(p + o2));
  return __builtin_bit_cast(bf16x8, __builtin_shufflevector(a, b, 0, 1, 2, 3, 4, 5, 6, 7));
}

#define GLU 1
#define CODE_P 10
#define FREE 3
#define K4MAP 0
#define GST 2
__device__ __forceinline__ void st16(char* p, u32x4 v) {
  asm volatile("global_store_dwordx4 %0, %1, off sc1\n\ts_nop 1" ::"v"(p), "v"(v) : "memory");
}
constexpr int G2_STAGE = 64 * 288, G2_LDS = 8 * G2_STAGE;

__device__ __forceinline__ void glds16_h(const void* base, u32 voff, u32 lds) {
  asm volatile("s_mov_b32 m0, %2\n\ts_mov_b32 exec_hi, 0\n\tglobal_load_lds_dwordx4 %0, %1\n\ts_mov_b32 exec_hi, -1" ::"v"(voff), "s"(base), "s"(lds) : "memory");
}
__device__ __forceinline__ constexpr int slab_row(int sg) { return 288 * (sg / 9) + (sg % 9 < 8 ? 72 * ((sg % 9) >> 1) + 32 * ((sg % 9) & 1) : 64); }

constexpr int G1_W1 = 16384, G1_W3 = G1_W1 + 9216 + 16, G1_STAGE = 35072, G1_LDS = 4 * G1_STAGE;

extern "C" __global__ __launch_bounds__(512, 2) void k4_kernel(const u16* __restrict__ w1, const u16* __restrict__ w3, const u16* __restrict__ w2,
                                                               char* __restrict__ ws, u16* __restrict__ out) {
  extern __shared__ __attribute__((aligned(16))) char smem[];
  const int tid = threadIdx.x, lane = tid & 63, wid = __builtin_amdgcn_readfirstlane(tid >> 6);
  STAMP_BEGIN();
  const int mt = (blockIdx.x >> 3) & 7, ft = (blockIdx.x & 7) * 4 + (blockIdx.x >> 6);
  const int wm = wid & 3, wn = wid >> 2;
  const char* tsrc = ws + WS_H2T + (size_t)(16 * mt + 2 * wid) * 18 * 1024;
  const char* wsrc = (const char*)(wn ? w3 : w1) + (size_t)(8 * wm) * (F * 2) + ft * 288;
  const int x1 = 64 + lane, x2 = 128 + (lane & 15);
  const u32 o0 = (lane / 18) * (F * 2) + (lane % 18) * 16, o1 = (x1 / 18) * (F * 2) + (x1 % 18) * 16, o2 = (x2 / 18) * (F * 2) + (x2 % 18) * 16;
  const u32 ol = lane * 16;
  const u32 tdst = lds_addr(smem) + 2 * wid * 1024, wdst = lds_addr(smem) + (wn ? G1_W3 : G1_W1) + wm * 2304;
  auto stage = [&](int p) {
    const u32 sb = (p & 3) * G1_STAGE;
    glds16(tsrc + p * 1024, ol, sb + tdst);
    glds16(tsrc + (18 + p) * 1024, ol, sb + tdst + 1024);
    const char* wp = wsrc + (size_t)p * 32 * (F * 2);
    glds16(wp, o0, sb + wdst);
    glds16(wp, o1, sb + wdst + 1024);
    glds16_q(wp, o2, sb + wdst + 2048);
  };
  const int g = lane >> 4, q = (lane >> 2) & 3, pc = lane & 3;
  const int boff = wm * 4096 + lane * 16;
  const int aoff = G1_W1 + (4 * g + q) * 288 + wn * 144 + 8 * pc;
  const int moff = (pc < 2 ? G1_W1 : G1_W3) + (4 * g + q) * 288 + wn * 144 + 128 + 8 * (pc & 1);
  f32x4 acc[9][4];
#pragma unroll
  for (int i = 0; i < 9; ++i)
#pragma unroll
    for (int j = 0; j < 4; ++j) acc[i][j] = f32x4{0, 0, 0, 0};

  stage(0); stage(1); stage(2);
  u32 pf = 0;
  const char* pfa;
  {
    constexpr int NL = (D - PF_ROWS) * 6 / 8;
    const int i = min(wm * 64 + lane, wn ? 161 : NL - 1);
    const int l = mt * NL + i, row = PF_ROWS + l / 6, c6 = l % 6;
    const char* p0 = (const char*)(c6 < 3 ? w1 : w3) + (size_t)row * (F * 2) + ((ft * 288) & ~127) + (c6 % 3) * 128;
    const char* p1 = (const char*)w2 + (size_t)(blockIdx.x * 162 + i) * 128;
    unsigned long long pc;
    u32 len;
    asm volatile("s_getpc_b64 %0\n.Lk4_here:\n\ts_mov_b32 %1, kz_end-.Lk4_here" : "=s"(pc), "=s"(len));
    const char* p2 = (const char*)pc + min((((blockIdx.x >> 3) & 7) * 64 + lane) * 128u, len - 4);
    pfa = wid == 7 ? p2 : wn ? p1 : p0;
  }
  WAIT_V(10); BAR();
  STAMP_MID(0);
  if (wn) BAR();
#pragma unroll
  for (int p = 0; p < 18; ++p) {
    const char* sb = smem + (p & 3) * G1_STAGE;
    bf16x8 A[9], B[4];
#pragma unroll
    for (int j = 0; j < 4; ++j) B[j] = *(const bf16x8*)(sb + boff + j * 1024);
#pragma unroll
    for (int i = 0; i < 4; ++i) {
      A[i] = tr_frag(sb + aoff + 32 * i, 16 * 288);
      A[4 + i] = tr_frag(sb + aoff + (G1_W3 - G1_W1) + 32 * i, 16 * 288);
    }
    A[8] = tr_frag(sb + moff, 16 * 288);
    if (p + 3 < 18) stage(p + 3);
    if (p < 7 ? wid == p : p == CODE_P && wid == 7) asm volatile("global_load_dword %0, %1, off" : "+v"(pf) : "v"(pfa) : "memory");
    if (p == CODE_P + 3) asm volatile("" ::"v"(pf));
    constexpr int NP = 18 - FREE;
    if (p < NP) {
      if (p < 15) WAIT_V(10); else if (p == 15) WAIT_V(5); else if (p == 16) WAIT_V(0);
      WAIT_L0(); SCHED();
      BAR();
      __builtin_amdgcn_s_setprio(1);
    }
#pragma unroll
    for (int i = 0; i < 9; ++i)
#pragma unroll
      for (int j = 0; j < 4; ++j) acc[i][j] = __builtin_amdgcn_mfma_f32_16x16x32_bf16(A[i], B[j], acc[i][j], 0, 0, 0);
    if (p < NP) __builtin_amdgcn_s_setprio(0);
    SCHED();
    if (p < NP - 1) BAR();
    else if (p == NP - 1) {
      if (wn) WAIT_V(0);
      if (FREE || !wn) BAR();
      if (FREE && !wn) { WAIT_V(0); BAR(); __builtin_amdgcn_s_setprio(2); }
    }
  }
  int tz = threadIdx.x;
  asm volatile("" : "+v"(tz));
  const int ln = tz & 63, g2 = ln >> 4, q2 = (ln >> 2) & 3, pc2 = ln & 3;
  const u32 ol2 = ln * 16;
  const int kq = blockIdx.x & 3, m2 = ((blockIdx.x >> 2) & 1) * 8 + ((blockIdx.x >> 3) & 7), cb = blockIdx.x >> 6;
  const int kg = wn, hs = wm >> 1;
  const char* gsrc = ws + WS_GT + ((size_t)(8 * m2 + 2 * wm) * 144 + 36 * kq + 18 * kg) * 1024;
  const char* w2src = (const char*)w2 + (size_t)(1152 * kq + 576 * kg) * (D * 2) + cb * 288;
  u32 wo[5];
#pragma unroll
  for (int i = 0; i < 5; ++i) { const int x = 64 * i + (i < 4 ? ln : ln & 31); wo[i] = (x / 18) * (D * 2) + (x % 18) * 16; }
  const u32 wo2l = wo[2] + (ln >= 16 ? 64 * (D * 2) : 0);
  const u32 w2dst = lds_addr(smem) + kg * 4 * G2_STAGE + wm * 4608;
  u32x4 B[4][4];
  auto issue = [&](int j, int i) {
    if (i < 5) {
      const bool left = hs ? (2 * j + 1) % 9 == 8 : (2 * j) % 9 == 8;
      const int row = (hs ? slab_row(2 * j + 1) : slab_row(2 * j)) + (left ? 144 : 16) * (wm & 1) + (left && i > 2 ? 64 : 0);
      const char* p = w2src + (size_t)row * (D * 2);
      const u32 dst = w2dst + (j & 3) * G2_STAGE + i * 1024;
      if (i == 4) glds16_h(p, wo[4], dst);
      else if (i == 2 && (j == 4 || j == 8)) glds16(p, left ? wo2l : wo[2], dst);
      else glds16(p, wo[i], dst);
    } else {
      const int f = i - 5;
      asm volatile("global_load_dwordx4 %0, %1, %2 sc1" : "=v"(B[j & 3][f]) : "v"(ol2), "s"(gsrc + ((size_t)(f & 1) * 144 + 2 * j + (f >> 1)) * 1024) : "memory");
    }
  };
  STAMP_MID(1);
  const int P = ft >> 1, qq = 2 * (ft & 1) + wn;
  char* gbase = ws + WS_GT + ((size_t)(16 * mt + 4 * wm) * 144 + 9 * P) * 1024;
  auto glu2 = [&](float g0, float g1, float u0, float u1) -> u32 {
    const f32x2 gv = {g0, g1}, uv = {u0, u1};
    const f32x2 t = gv * -1.4426950408889634f;
    const f32x2 dn = f32x2{__builtin_amdgcn_exp2f(t[0]), __builtin_amdgcn_exp2f(t[1])} + 1.0f;
    const f32x2 o = (gv * uv) * f32x2{__builtin_amdgcn_rcpf(dn[0]), __builtin_amdgcn_rcpf(dn[1])};
    return pk_bf(o[0], o[1]);
  };
#pragma unroll
  for (int j = 0; j < 4; ++j)
#pragma unroll
    for (int s = 0; s < 2; ++s) {
      u32x4 o;
#pragma unroll
      for (int h = 0; h < 2; ++h) {
        const f32x4 a = acc[2 * s + h][j], u = acc[4 + 2 * s + h][j];
        o[2 * h] = glu2(a[0], a[1], u[0], u[1]);
        o[2 * h + 1] = glu2(a[2], a[3], u[2], u[3]);
      }
      st16(gbase + (size_t)j * 144 * 1024 + (2 * qq + s) * 1024 + ln * 16, o);
    }
#pragma unroll
  for (int jp = 0; jp < 2; ++jp) {
    float a[4], u[4];
#pragma unroll
    for (int e = 0; e < 4; ++e) {
      const float m0 = acc[8][2 * jp][e], m1 = acc[8][2 * jp + 1][e];
      auto r = __builtin_amdgcn_permlane32_swap(__builtin_bit_cast(u32, m0), __builtin_bit_cast(u32, m1), false, false);
      a[e] = __builtin_bit_cast(float, (u32)r[0]); u[e] = __builtin_bit_cast(float, (u32)r[1]);
    }
    const u32x2 o = {glu2(a[0], a[1], u[0], u[1]), glu2(a[2], a[3], u[2], u[3])};
    const int j = 2 * jp + (ln >> 5);
    char* gp = gbase + (size_t)j * 144 * 1024 + 8 * 1024 + (16 * (2 * (qq & 1) + (g2 & 1)) + (ln & 15)) * 16 + 8 * (qq >> 1);
    asm volatile("global_store_dwordx2 %0, %1, off sc1\n\ts_nop 1" ::"v"(gp), "v"(o) : "memory");
  }
  __builtin_amdgcn_s_setprio(0);
  WAIT_V(0); BAR();
  STAMP_MID(2);
  {
    u32* gfl = (u32*)(ws + WS_FLAG) + 8192;
    if (wid == 0) {
      if (ln == 0) __hip_atomic_store(gfl + (mt * 4 + (ft >> 3)) * 32 + (ft & 7), 1u, __ATOMIC_RELAXED, __HIP_MEMORY_SCOPE_AGENT);
    }
#pragma unroll
    for (int j = 0; j < 2; ++j)
#pragma unroll
      for (int i = 0; i < 5; ++i) issue(j, i);
    if (wid == 1) {
#pragma unroll 1
      for (int spin = 0; spin < (1 << 20); ++spin) {
        u32 f;
        asm volatile("global_load_dword %0, %1, off sc1\n\ts_waitcnt vmcnt(0)" : "=v"(f) : "v"(gfl + ((m2 >> 1) * 4 + kq) * 32 + (ln & 7)) : "memory");
        if (__builtin_amdgcn_ballot_w64(f != 1u) == 0) break;
      }
    }
    BAR();
  }
#define WAIT_B(n, j) asm volatile("s_waitcnt vmcnt(" #n ")" : "+v"(B[(j) & 3][0]), "+v"(B[(j) & 3][1]), "+v"(B[(j) & 3][2]), "+v"(B[(j) & 3][3])::"memory")
  const char* abase = smem + kg * 4 * G2_STAGE + (4 * g2 + q2) * 288 + 8 * pc2;
  f32x4 ac2[9][2];
#pragma unroll
  for (int c = 0; c < 9; ++c) ac2[c][0] = ac2[c][1] = f32x4{0, 0, 0, 0};
#pragma unroll
  for (int j = 0; j < 2; ++j)
#pragma unroll
    for (int i = 5; i < 9; ++i) issue(j, i);
  WAIT_B(4, 0); BAR();
  STAMP_MID(3);
  if (kg) BAR();
#pragma unroll
  for (int j = 0; j < 9; ++j) {
    bf16x8 A[2][9];
#pragma unroll
    for (int h = 0; h < 2; ++h)
#pragma unroll
      for (int c = 0; c < 9; ++c) A[h][c] = tr_frag(abase + (j & 3) * G2_STAGE + h * 32 * 288 + 32 * c, 16 * 288);
    WAIT_L0(); SCHED();
    BAR();
    __builtin_amdgcn_s_setprio(1);
#pragma unroll
    for (int i = 0; i < 9; ++i) {
#pragma unroll
      for (int e = 0; e < 2; ++e) {
        const int h = (2 * i + e) / 9, c = (2 * i + e) % 9;
#pragma unroll
        for (int t = 0; t < 2; ++t) {
          const u32x4 bv = B[j & 3][2 * h + t];
          ac2[c][t] = __builtin_amdgcn_mfma_f32_16x16x32_bf16(A[h][c], __builtin_bit_cast(bf16x8, bv), ac2[c][t], 0, 0, 0);
        }
      }
      SCHED();
      if (j + 2 < 9) issue(j + 2, i);
      SCHED();
    }
    __builtin_amdgcn_s_setprio(0);
    if (j < 7) WAIT_B(9, j + 1); else if (j == 7) WAIT_B(0, j + 1);
    SCHED();
    if (j < 8 || !kg) BAR();
  }
  STAMP_MID(4);
  const int et = kg, ep = wm;
  const size_t orow = (size_t)(128 * m2 + 32 * kq + 16 * et + (ln & 15)) * D + 144 * cb + 4 * g2;
  f32x4 gb[3]; u32x2 xr[3];
#pragma unroll
  for (int c = 0; c < 3; ++c) {
    const int ct = c < 2 ? 2 * ep + c : 8;
    gb[c] = *(const f32x4*)((const float*)(ws + WS_MODF) + 5 * D + 144 * cb + 16 * ct + 4 * g2);
    xr[c] = *(const u32x2*)((const u16*)(ws + WS_X1) + orow + 16 * ct);
  }
  f32x4* red = (f32x4*)smem;
#pragma unroll
  for (int c = 0; c < 9; ++c) red[(wid * 9 + c) * 64 + ln] = kg ? ac2[c][0] : ac2[c][1];
  WAIT_L0(); BAR();
  const int tile = m2 * 4 + cb;
  char* xch = ws + WS_XCH + (size_t)tile * (12 * 9216);
  f32x4* own = red + 72 * 64;
  {
    f32x4 v[9];
#pragma unroll
    for (int c = 0; c < 9; ++c) v[c] = (kg ? ac2[c][1] : ac2[c][0]) + red[((wid ^ 4) * 9 + c) * 64 + ln];
    if (wm == kq) {
#pragma unroll
      for (int c = 0; c < 9; ++c) own[(kg * 9 + c) * 64 + ln] = v[c];
    } else {
      char* p = xch + ((wm * 3 + (kq > wm ? kq - 1 : kq)) * 2 + kg) * 4608;
#pragma unroll
      for (int c = 0; c < 4; ++c) {
        const u32x4 w = {pk_bf(v[2 * c][0], v[2 * c][1]), pk_bf(v[2 * c][2], v[2 * c][3]), pk_bf(v[2 * c + 1][0], v[2 * c + 1][1]),
                         pk_bf(v[2 * c + 1][2], v[2 * c + 1][3])};
        asm volatile("global_store_dwordx4 %0, %1, off sc1\n\ts_nop 1" ::"v"(p + c * 1024 + ln * 16), "v"(w) : "memory");
      }
      const u32x2 w = {pk_bf(v[8][0], v[8][1]), pk_bf(v[8][2], v[8][3])};
      asm volatile("global_store_dwordx2 %0, %1, off sc1\n\ts_nop 1" ::"v"(p + 4096 + ln * 8), "v"(w) : "memory");
    }
  }
  asm volatile("s_waitcnt vmcnt(0) lgkmcnt(0)" : "+v"(gb[0]), "+v"(gb[1]), "+v"(gb[2]), "+v"(xr[0]), "+v"(xr[1]), "+v"(xr[2])::"memory");
  BAR();
  u32* flags = (u32*)(ws + WS_FLAG) + tile * 128;
  if (wid == 0) {
    if (ln == 0) {
#pragma unroll
      for (int r = 0; r < 4; ++r)
        if (r != kq) __hip_atomic_store(flags + r * 32 + (kq > r ? kq - 1 : kq), 1u, __ATOMIC_RELAXED, __HIP_MEMORY_SCOPE_AGENT);
    }
  } else if (wid == 1) {
#pragma unroll 1
    for (int spin = 0; spin < (1 << 20); ++spin) {
      u32x4 f;
      asm volatile("global_load_dwordx4 %0, %1, off sc1\n\ts_waitcnt vmcnt(0)" : "=v"(f) : "v"(flags + kq * 32) : "memory");
      if (__builtin_amdgcn_readfirstlane(f[0] & f[1] & f[2]) == 1u) break;
    }
  }
  BAR();
  STAMP_MID(5);
  {
    u32x4 pr[3]; u32x2 pl[3];
    const char* src = xch + ((size_t)kq * 6 + et) * 4608;
    const char *s4 = src + ep * 1024 + ln * 16, *s2 = src + 4096 + ln * 8;
    asm volatile("global_load_dwordx4 %0, %6, off sc1\n\tglobal_load_dwordx4 %1, %7, off sc1\n\tglobal_load_dwordx4 %2, %8, off sc1\n\t"
                 "global_load_dwordx2 %3, %9, off sc1\n\tglobal_load_dwordx2 %4, %10, off sc1\n\tglobal_load_dwordx2 %5, %11, off sc1\n\t"
                 "s_waitcnt vmcnt(0)"
                 : "=&v"(pr[0]), "=&v"(pr[1]), "=&v"(pr[2]), "=&v"(pl[0]), "=&v"(pl[1]), "=&v"(pl[2])
                 : "v"(s4), "v"(s4 + 9216), "v"(s4 + 18432), "v"(s2), "v"(s2 + 9216), "v"(s2 + 18432) : "memory");
#pragma unroll
    for (int c = 0; c < 3; ++c) {
      const int ct = c < 2 ? 2 * ep + c : 8;
      f32x4 s = own[(et * 9 + ct) * 64 + ln];
#pragma unroll
      for (int k = 0; k < 3; ++k) {
        const u32 lo = c < 2 ? pr[k][2 * c] : pl[k][0], hi = c < 2 ? pr[k][2 * c + 1] : pl[k][1];
        s += f32x4{bflo(lo), bfhi(lo), bflo(hi), bfhi(hi)};
      }
      u32x2 o;
#pragma unroll
      for (int h = 0; h < 2; ++h) {
        const u32 y = pk_bf(s[2 * h], s[2 * h + 1]);
        const u32 z = pk_bf(gb[c][2 * h] * bflo(y), gb[c][2 * h + 1] * bfhi(y));
        o[h] = pk_bf(bflo(xr[c][h]) + bflo(z), bfhi(xr[c][h]) + bfhi(z));
      }
      if (c < 2 || ep == 0) *(u32x2*)(out + orow + 16 * ct) = o;
    }
  }
  STAMP_END(3);
}

extern "C" __device__ __attribute__((used, noinline)) void kz_end() {}

extern "C" void kernel_launch(void* const* d_in, const int* in_sizes, int n_in, void* d_out, int out_size, void* d_ws, size_t ws_size,
                              hipStream_t stream) {
  const u16* u = (const u16*)d_in[0];
  char* ws = (char*)d_ws;
  (void)hipFuncSetAttribute((const void*)k4_kernel, hipFuncAttributeMaxDynamicSharedMemorySize, G2_LDS);
  k1_kernel<<<K1_MOD + K1_LN + K1_KG, 256, 0, stream>>>(u, (const u16*)d_in[1], (const u16*)d_in[2], (const u16*)d_in[3], (const u16*)d_in[4],
                                                        (const u16*)d_in[5], (const u16*)d_in[6], (const u16*)d_in[7], (const u16*)d_in[13],
                                                        (const u16*)d_in[14], ws);
  k2_kernel<<<D, 256, 0, stream>>>((const u16*)d_in[8], (const u16*)d_in[10], (const u16*)d_in[11], ws);
  k3_kernel<<<256, 576, 0, stream>>>(u, ws);
  k4_kernel<<<256, 512, G2_LDS, stream>>>((const u16*)d_in[10], (const u16*)d_in[11], (const u16*)d_in[12], ws, (u16*)d_out);
}
```

```cpp
#include <hip/hip_runtime.h>
#include <hip/hip_bf16.h>

typedef unsigned short u16;
typedef unsigned int u32;
typedef __attribute__((ext_vector_type(2))) float f32x2;
typedef __attribute__((ext_vector_type(4))) float f32x4;
typedef __attribute__((ext_vector_type(2))) u32 u32x2;
typedef __attribute__((ext_vector_type(4))) u32 u32x4;
typedef __attribute__((ext_vector_type(4))) short s16x4;
typedef __attribute__((ext_vector_type(8))) short s16x8;
typedef __attribute__((ext_vector_type(2))) __bf16 bf16x2;
typedef __attribute__((ext_vector_type(8))) __bf16 bf16x8;
typedef __attribute__((ext_vector_type(2))) _Float16 f16x2;
typedef __attribute__((ext_vector_type(8))) _Float16 f16x8;
#define LAS __attribute__((address_space(3)))
#define GAS __attribute__((address_space(1)))

constexpr int L = 2048, D = 576, NM = 128, F = 4608, MODN = 6 * D;
constexpr int KSPLIT = 3;

constexpr size_t WS_MODP = 0;
constexpr size_t WS_MODF = 65536;
constexpr size_t WS_XHT  = 131072;
constexpr size_t ACT_B   = (size_t)L * D * 2;
constexpr size_t WS_KX   = WS_XHT + ACT_B;
constexpr size_t WS_RT   = WS_KX + ACT_B;
constexpr size_t WS_X1   = WS_RT + ACT_B;
constexpr size_t WS_H2T  = WS_X1 + ACT_B;
constexpr size_t WS_GT   = WS_H2T + ACT_B;
constexpr size_t WS_XCH  = WS_GT + (size_t)L * F * 2;
constexpr size_t WS_FLAG = 81920;

#define WAIT_V(n) asm volatile("s_waitcnt vmcnt(" #n ")" ::: "memory")
#define WAIT_L0() asm volatile("s_waitcnt lgkmcnt(0)" ::: "memory")
#define BAR() __builtin_amdgcn_s_barrier()
#define SCHED() __builtin_amdgcn_sched_barrier(0)
#define STAMP_BEGIN()
#define STAMP_MID(i)
#define STAMP_END(k)
__device__ __forceinline__ float bf2f(u16 v) { return __builtin_bit_cast(float, (u32)v << 16); }
__device__ __forceinline__ float bflo(u32 v) { return __builtin_bit_cast(float, v << 16); }
__device__ __forceinline__ float bfhi(u32 v) { return __builtin_bit_cast(float, v & 0xffff0000u); }
__device__ __forceinline__ u32 pk_bf(float a, float b) { f32x2 v = {a, b}; return __builtin_bit_cast(u32, __builtin_convertvector(v, bf16x2)); }
__device__ __forceinline__ u32 pk_h(float a, float b) { f32x2 v = {a, b}; return __builtin_bit_cast(u32, __builtin_convertvector(v, f16x2)); }
__device__ __forceinline__ float bfr(float a) { return bflo(pk_bf(a, a)); }

template <int CTRL> __device__ __forceinline__ float dpp(float v) {
  return __builtin_bit_cast(float, __builtin_amdgcn_update_dpp(0, __builtin_bit_cast(int, v), CTRL, 0xf, 0xf, true));
}
__device__ __forceinline__ float sum16x(float v) {
  u32 u = __builtin_bit_cast(u32, v);
  auto r = __builtin_amdgcn_permlane16_swap(u, u, false, false);
  return __builtin_bit_cast(float, (u32)r[0]) + __builtin_bit_cast(float, (u32)r[1]);
}
__device__ __forceinline__ float sum32x(float v) {
  u32 u = __builtin_bit_cast(u32, v);
  auto r = __builtin_amdgcn_permlane32_swap(u, u, false, false);
  return __builtin_bit_cast(float, (u32)r[0]) + __builtin_bit_cast(float, (u32)r[1]);
}
__device__ __forceinline__ float wave_sum(float v) {
  v += dpp<0xB1>(v);
  v += dpp<0x4E>(v);
  v += dpp<0x141>(v);
  v += dpp<0x140>(v);
  return sum32x(sum16x(v));
}
__device__ __forceinline__ float sum_hi3(float v) {
  v += dpp<0x128>(v);
  return sum32x(sum16x(v));
}
__device__ __forceinline__ float silu_f(float g) { return g * __builtin_amdgcn_rcpf(1.0f + __builtin_amdgcn_exp2f(-1.4426950408889634f * g)); }

__device__ __forceinline__ u32 touch(const void* p) {
  u32 r;
  asm volatile("global_load_dword %0, %1, off" : "=v"(r) : "v"(p) : "memory");
  return r;
}
#define TOUCH_DONE(r) asm volatile("s_waitcnt vmcnt(0)" : "+v"(r)::"memory")

constexpr int K1_MOD = 54 * KSPLIT, K1_LN = 256, K1_KG = D;
constexpr int TROW = 528;

typedef f32x2 cplx;
__device__ __forceinline__ cplx cmul(cplx a, cplx z) {
  cplx t, r;
  asm("v_pk_mul_f32 %0, %1, %2 op_sel_hi:[1,0]" : "=v"(t) : "v"(a), "v"(z));
  asm("v_pk_fma_f32 %0, %1, %2, %3 op_sel:[1,1,0] op_sel_hi:[0,1,1] neg_lo:[1,0,0]" : "=v"(r) : "v"(a), "v"(z), "v"(t));
  return r;
}
__device__ __forceinline__ cplx cexpl(float al2, float brev, float l) {
  const float m = __builtin_amdgcn_exp2f(al2 * l);
  const float ph = __builtin_amdgcn_fractf(brev * l);
  return cplx{m * __builtin_amdgcn_cosf(ph), m * __builtin_amdgcn_sinf(ph)};
}

extern "C" __global__ __launch_bounds__(256) void k1_kernel(const u16* __restrict__ x, const u16* __restrict__ Are, const u16* __restrict__ Aim,
                                                 const u16* __restrict__ Bre, const u16* __restrict__ Bim, const u16* __restrict__ Cre,
                                                 const u16* __restrict__ Cim, const u16* __restrict__ logdt, const u16* __restrict__ cvec,
                                                 const u16* __restrict__ wmod, char* __restrict__ ws) {
  __shared__ __attribute__((aligned(16))) char smem[64 * TROW + 2624];
  STAMP_BEGIN();
  const int tid = threadIdx.x, lane = tid & 63, wid = tid >> 6;
  int b = blockIdx.x;
  u32 pf = 0;
  if (b < 16) {
    unsigned long long pc;
    u32 len;
    asm volatile("s_getpc_b64 %0\n.Lk1_here:\n\ts_mov_b32 %1, kz_end-.Lk1_here" : "=s"(pc), "=s"(len));
    const u32 o = ((b >> 3) * 256 + tid) * 128;
    if (o < len) pf = touch((const void*)(pc + o));
  }
#define K1_DONE() TOUCH_DONE(pf)
  b = b < K1_KG ? b + K1_MOD + K1_LN : b - K1_KG;
  if (b < K1_MOD) {
    const int cb = b % 54, kq = b / 54, cc = tid & 7, rg = tid >> 3;
    u32x4 w[6]; u16 cv[6];
#pragma unroll
    for (int i = 0; i < 6; ++i) {
      const int row = 192 * kq + rg + 32 * i;
      w[i] = *(const u32x4*)(wmod + (size_t)row * MODN + 64 * cb + 8 * cc);
      cv[i] = cvec[row];
    }
    float acc[8] = {0, 0, 0, 0, 0, 0, 0, 0};
#pragma unroll
    for (int i = 0; i < 6; ++i) {
      const float s = bfr(silu_f(bf2f(cv[i])));
#pragma unroll
      for (int j = 0; j < 4; ++j) { acc[2 * j] += s * bflo(w[i][j]); acc[2 * j + 1] += s * bfhi(w[i][j]); }
    }
#pragma unroll
    for (int j = 0; j < 8; ++j) acc[j] = sum_hi3(acc[j]);
    float* red = (float*)smem;
    if (lane < 8) {
      *(f32x4*)(red + wid * 64 + lane * 8) = f32x4{acc[0], acc[1], acc[2], acc[3]};
      *(f32x4*)(red + wid * 64 + lane * 8 + 4) = f32x4{acc[4], acc[5], acc[6], acc[7]};
    }
    __syncthreads();
    if (tid < 64) ((float*)(ws + WS_MODP))[((cb % 9) * 64 + tid) * 24 + (cb / 9) * 4 + kq] = (red[tid] + red[64 + tid]) + (red[128 + tid] + red[192 + tid]);
    STAMP_END(0);
    return;
  }
  b -= K1_MOD;
  if (b < K1_LN) {
    u32 v[2][4]; u16 t[2];
#pragma unroll
    for (int r = 0; r < 2; ++r) {
      const u16* row = x + (size_t)(8 * b + 2 * wid + r) * D;
#pragma unroll
      for (int i = 0; i < 4; ++i) v[r][i] = *(const u32*)(row + 2 * (lane + 64 * i));
      t[r] = row[512 + lane];
    }
    u32 o[2][5];
#pragma unroll
    for (int r = 0; r < 2; ++r) {
      float e[9];
#pragma unroll
      for (int i = 0; i < 4; ++i) { e[2 * i] = bflo(v[r][i]); e[2 * i + 1] = bfhi(v[r][i]); }
      e[8] = bf2f(t[r]);
      float s = 0;
#pragma unroll
      for (int i = 0; i < 9; ++i) s += e[i];
      const float mean = wave_sum(s) * (1.0f / D);
      float q = 0;
#pragma unroll
      for (int i = 0; i < 9; ++i) { const float dlt = e[i] - mean; q += dlt * dlt; }
      const float var = wave_sum(q) * (1.0f / D);
      const float m = bfr(mean);
      const float rs = __builtin_amdgcn_rcpf(bfr(__builtin_amdgcn_sqrtf(bfr(bfr(var) + 1e-5f))));
#pragma unroll
      for (int i = 0; i < 4; ++i) {
        const u32 dd = pk_bf(e[2 * i] - m, e[2 * i + 1] - m);
        o[r][i] = pk_bf(bflo(dd) * rs, bfhi(dd) * rs);
      }
      o[r][4] = pk_bf(bfr(e[8] - m) * rs, 0.f);
    }
    u32* tile = (u32*)smem;
#pragma unroll
    for (int i = 0; i < 4; ++i) {
      tile[(2 * (lane + 64 * i)) * 4 + wid] = __builtin_amdgcn_perm(o[1][i], o[0][i], 0x05040100);
      tile[(2 * (lane + 64 * i) + 1) * 4 + wid] = __builtin_amdgcn_perm(o[1][i], o[0][i], 0x07060302);
    }
    tile[(512 + lane) * 4 + wid] = __builtin_amdgcn_perm(o[1][4], o[0][4], 0x05040100);
    __syncthreads();
    u32x4* dst = (u32x4*)(ws + WS_XHT) + (size_t)b * D;
#pragma unroll
    for (int i = 0; i < 3; ++i) {
      const int c = tid + 256 * i;
      if (c < D) dst[c] = *(const u32x4*)(tile + c * 4);
    }
    STAMP_END(0);
    return;
  }
  b -= K1_LN;
  {
    const int n = tid & 127, half = __builtin_amdgcn_readfirstlane(tid >> 7);
    const int idx = b * NM + n;
    const float are = bf2f(Are[idx]), aim = bf2f(Aim[idx]), bre = bf2f(Bre[idx]), bim = bf2f(Bim[idx]);
    const float cre = bf2f(Cre[idx]), cim = bf2f(Cim[idx]);
    const float dt = bfr(__builtin_amdgcn_exp2f(1.4426950408889634f * bf2f(logdt[b])));
    const float al2 = (dt * are) * 1.4426950408889634f, brev = (dt * aim) * 0.15915494309189535f;
    cplx c = cmul(cplx{cre, cim}, cplx{dt * bre, dt * bim});
    const int he = (__builtin_amdgcn_frexp_expf(fmaxf(fabsf(c[0]), fabsf(c[1])))) >> 1;
    c = cplx{__builtin_amdgcn_ldexpf(c[0], -he), __builtin_amdgcn_ldexpf(c[1], -he)};
    char* Ut = smem + n * 4;
#define PUT(row, z) (*(u32*)(Ut + (row) * TROW) = pk_h((z)[0], (z)[1]))
    const cplx z128 = cexpl(al2, brev, 128.f);
    if (half == 0) {
      const cplx z8c = cexpl(al2, -brev, 8.f);
      cplx v = {__builtin_amdgcn_ldexpf(1.0f, he), 0.f};
      cplx u = cmul(c, cexpl(al2, brev, 1024.f));
#pragma unroll
      for (int j = 0; j < 16; ++j) {
        PUT(48 + j, v); if (j < 15) v = cmul(v, z8c);
        if (j < 9) { PUT(32 + j, u); if (j < 8) u = cmul(u, z128); }
      }
      const cplx bd = {fabsf(c[0]) + fabsf(c[1]), 0.f};
      PUT(41, bd);
    } else {
      const cplx z1 = cexpl(al2, brev, 1.f);
      cplx u[8];
      u[0] = c;
#pragma unroll
      for (int j = 1; j < 8; ++j) u[j] = cmul(u[j - 1], z1);
#pragma unroll
      for (int j = 0; j < 8; ++j) { PUT(j, u[j]); u[j] = cmul(u[j], z128); PUT(8 + j, u[j]); }
#pragma unroll
      for (int s = 0; s < 2; ++s)
#pragma unroll
        for (int j = 0; j < 4; ++j) { u[2 * j] = cmul(u[2 * j], z128); PUT(16 + 4 * s + j, u[2 * j]); }
#pragma unroll
      for (int s = 0; s < 4; ++s)
#pragma unroll
        for (int j = 0; j < 2; ++j) { u[4 * j] = cmul(u[4 * j], z128); PUT(24 + 2 * s + j, u[4 * j]); }
    }
#undef PUT
    WAIT_L0(); BAR();
    float* Kc = (float*)(smem + 64 * TROW);
    const int wu = __builtin_amdgcn_readfirstlane(wid);
    if (wu < 3) {
      const int r = lane & 15, g = lane >> 4;
      f32x4 acc = {0, 0, 0, 0};
#pragma unroll
      for (int ks = 0; ks < 8; ++ks) {
        const f16x8 a = *(const f16x8*)(smem + (16 * wu + r) * TROW + ks * 64 + g * 16);
        const f16x8 v = *(const f16x8*)(smem + (48 + r) * TROW + ks * 64 + g * 16);
        acc = __builtin_amdgcn_mfma_f32_16x16x32_f16(a, v, acc, 0, 0, 0);
      }
      int o0, sA = 1, sB;
      if (wu == 0) { o0 = (g < 2 ? 4 * g : 120 + 4 * g) + 8 * r; sB = 2; }
      else if (wu == 1) { o0 = g < 2 ? 256 + 64 * g + 4 * r : 256 + 64 * g + 2 * r; sB = g < 2 ? 2 : 32; }
      else { o0 = g < 2 ? 512 + 64 * g + r : (lane == 32 ? 640 : 648); sA = g < 2 ? 16 : 1; sB = g < 2 ? 32 : 4; }
      Kc[o0] = acc[0]; Kc[o0 + sA] = acc[1]; Kc[o0 + sB] = acc[2]; Kc[o0 + sA + sB] = acc[3];
    }
    WAIT_L0(); BAR();
    {
      const int q = tid;
      float k[8];
      if (q < 32) {
#pragma unroll
        for (int e = 0; e < 8; ++e) k[e] = Kc[8 * q + e];
      } else if (q < 64) {
        const float* p = Kc + 256 + 4 * (q - 32);
        k[0] = k[1] = p[0]; k[2] = p[1]; k[3] = k[4] = k[5] = p[2]; k[6] = p[3]; k[7] = p[4];
      } else if (q < 128) {
        const float* p = Kc + 384 + 2 * (q - 64);
        k[0] = k[1] = k[2] = p[0]; k[3] = k[4] = k[5] = p[1]; k[6] = k[7] = p[2];
      } else {
        const float* p = Kc + 512 + (q - 128);
        k[0] = k[1] = k[2] = k[3] = p[0]; k[5] = k[6] = k[7] = p[1]; k[4] = (q & 1) ? p[1] : p[0];
      }
      const int se = 14 - __builtin_amdgcn_frexp_expf(Kc[641]);
      u32x4 o;
#pragma unroll
      for (int e = 0; e < 4; ++e) o[e] = pk_h(__builtin_amdgcn_ldexpf(k[7 - 2 * e], se), __builtin_amdgcn_ldexpf(k[6 - 2 * e], se));
      ((u32x4*)(ws + WS_KX))[(size_t)b * 256 + q] = o;
      if (tid == 0) ((int*)(ws + WS_MODP))[b * 24 + 3] = se;
    }
    K1_DONE();
    STAMP_END(0);
  }
}

#define PF_ROWS 288
constexpr int KCP = 4160;
constexpr int K2_H = 4 * KCP;
constexpr int K2_RED = K2_H + 4608;

extern "C" __global__ __launch_bounds__(256) void k2_kernel(const u16* __restrict__ dskip, const u16* __restrict__ w1, const u16* __restrict__ w3,
                                                            char* __restrict__ ws) {
  __shared__ __attribute__((aligned(16))) char smem[K2_RED + 24 * 1024];
  STAMP_BEGIN();
  const int tid = threadIdx.x, lane = tid & 63, wid = __builtin_amdgcn_readfirstlane(tid >> 6);
  const int d = (blockIdx.x & 7) * 72 + (blockIdx.x >> 3);
  const u32x4* kx = (const u32x4*)(ws + WS_KX) + (size_t)d * 256;
  const u32x4 xh = ((const u32x4*)(ws + WS_XHT))[(size_t)tid * D + d];
  const u32x4 kA = kx[tid];
  u32x2 kB = *(const u32x2*)(kx + max(tid - 1, 0));
  const f32x4* mp = (const f32x4*)(ws + WS_MODP) + d * 6;
  float md[6];
  int se = 0;
#pragma unroll
  for (int i = 0; i < 6; ++i) {
    const f32x4 v = mp[i];
    md[i] = bfr(v[0] + v[1] + v[2]);
    if (i == 0) { const float e = v[3]; se = __builtin_bit_cast(int, e); }
  }
  md[1] = bfr(1.0f + md[1]); md[4] = bfr(1.0f + md[4]);
  const u32 dkw = ((const u32*)dskip)[d >> 1];
  const float dk = (d & 1) ? bfhi(dkw) : bflo(dkw);
  const float sa = md[0], c1 = md[1], ga = md[2];
  const int he = 14 - __builtin_amdgcn_frexp_expf(24.5f * fabsf(c1) + fabsf(sa));
  if (tid == 0) kB = u32x2{0, 0};
  {
    char* p = smem + (255 - tid) * 16;
    const u32 e0 = __builtin_amdgcn_alignbit(kA[1], kA[0], 16), e1 = __builtin_amdgcn_alignbit(kA[2], kA[1], 16);
    const u32 e2 = __builtin_amdgcn_alignbit(kA[3], kA[2], 16), e3 = __builtin_amdgcn_alignbit(kB[0], kA[3], 16);
    const u32 e4 = __builtin_amdgcn_alignbit(kB[1], kB[0], 16);
    *(u32x4*)(p + 0 * KCP) = u32x4{e1, e2, e3, e4};
    *(u32x4*)(p + 1 * KCP) = u32x4{kA[1], kA[2], kA[3], kB[0]};
    *(u32x4*)(p + 2 * KCP) = u32x4{e0, e1, e2, e3};
    *(u32x4*)(p + 3 * KCP) = kA;
    if (tid < 8) {
      *(u32x4*)(smem + (tid & 3) * KCP + (256 + (tid >> 2)) * 16) = u32x4{0, 0, 0, 0};
    }
    u32x4 hh;
#pragma unroll
    for (int i = 0; i < 4; ++i) {
      const u32 m = pk_bf(bflo(xh[i]) * c1, bfhi(xh[i]) * c1);
      const u32 h = pk_bf(bflo(m) + sa, bfhi(m) + sa);
      hh[i] = pk_h(__builtin_amdgcn_ldexpf(bflo(h), he), __builtin_amdgcn_ldexpf(bfhi(h), he));
    }
    *(u32x4*)(smem + K2_H + 512 + tid * 16) = hh;
    if (tid < 32) *(u32x4*)(smem + K2_H + tid * 16) = u32x4{0, 0, 0, 0};
  }
  u32 pf = 0;
  {
    const int i = blockIdx.x * 256 + tid;
    if (i < 2 * PF_ROWS * 72) pf = touch((const char*)(i < PF_ROWS * 72 ? w1 : w3) + (size_t)(i < PF_ROWS * 72 ? i : i - PF_ROWS * 72) * 128);
  }
  STAMP_MID(0);
  WAIT_L0(); BAR();
  STAMP_MID(1);
  if (tid == 0) {
    float* mf = (float*)(ws + WS_MODF);
#pragma unroll
    for (int i = 3; i < 6; ++i) mf[i * D + d] = md[i];
  }
  const int r = lane & 15, g = lane >> 4;
  f32x4 acc[8];
#pragma unroll
  for (int i = 0; i < 8; ++i) acc[i] = f32x4{0, 0, 0, 0};
  const char* abase = smem + (r & 3) * KCP + 2 * (2044 + 8 - 8 * g - (r & 12));
  const char* bbase = smem + K2_H + 512 + 2 * (16 * r - 8 * g + 8);
#pragma unroll
  for (int cls = 0; cls < 2; ++cls) {
    const int i0 = 2 * wid + cls;
    const char* ap = abase - 64 * (7 - i0);
    const char* bp = bbase + 64 * (i0 - 7);
    f16x8 A[8], B[8];
#pragma unroll
    for (int m = 0; m < 8; ++m) {
      const u32x2 lo = *(const volatile LAS u32x2*)(ap - 512 * m), hi = *(const volatile LAS u32x2*)(ap - 512 * m + 8);
      A[m] = __builtin_bit_cast(f16x8, u32x4{lo[0], lo[1], hi[0], hi[1]});
      B[m] = *(const f16x8*)(bp + 512 * m);
    }
#pragma unroll
    for (int n = 0; n < 8; ++n)
#pragma unroll
      for (int k = 0; k <= n; ++k) acc[n] = __builtin_amdgcn_mfma_f32_16x16x32_f16(A[n - k], B[k], acc[n], 0, 0, 0);
  }
  STAMP_MID(2);
  f32x4* red = (f32x4*)(smem + K2_RED);
#pragma unroll
  for (int n = 0; n < 8; ++n) {
    const int o = n >> 1;
    if (o != wid) red[(n * 3 + (wid > o ? wid - 1 : wid)) * 64 + lane] = acc[n];
  }
  WAIT_L0(); BAR();
  STAMP_MID(3);
#pragma unroll
  for (int e = 0; e < 2; ++e) {
    const int n = 2 * wid + e;
    f32x4 s = wid == 0 ? acc[e] : wid == 1 ? acc[2 + e] : wid == 2 ? acc[4 + e] : acc[6 + e];
#pragma unroll
    for (int j = 0; j < 3; ++j) s += red[(n * 3 + j) * 64 + lane];
    const int t = 256 * n + 16 * r + 4 * g;
    const u32x2 hv = *(const u32x2*)(smem + K2_H + 512 + 2 * t);
    u32x2 o;
#pragma unroll
    for (int j = 0; j < 2; ++j) {
      const u32 hw = hv[j];
      const f16x2 hp = __builtin_bit_cast(f16x2, hw);
      const u32 y = pk_bf(__builtin_amdgcn_ldexpf(s[2 * j], -se - he), __builtin_amdgcn_ldexpf(s[2 * j + 1], -se - he));
      const u32 sk = pk_bf(__builtin_amdgcn_ldexpf((float)hp[0], -he) * dk, __builtin_amdgcn_ldexpf((float)hp[1], -he) * dk);
      const u32 ys = pk_bf(bflo(y) + bflo(sk), bfhi(y) + bfhi(sk));
      o[j] = pk_bf(ga * bflo(ys), ga * bfhi(ys));
    }
    *(u32x2*)(ws + WS_RT + ((size_t)(t >> 3) * D + d) * 16 + (t & 4) * 2) = o;
  }
  TOUCH_DONE(pf);
  STAMP_END(1);
}

extern "C" __global__ __launch_bounds__(576) void k3_kernel(const u16* __restrict__ x, char* __restrict__ ws) {
  __shared__ float red[2][9][8];
  STAMP_BEGIN();
  const int tid = threadIdx.x, lane = tid & 63, wid = tid >> 6;
  const int tq = (blockIdx.x & 7) * 32 + (blockIdx.x >> 3);
  if (tid < 36) ((u32*)(ws + WS_FLAG))[blockIdx.x * 36 + tid] = 0;
  const int tok = lane & 7, piece = tid >> 3, ks = piece >> 2, g = piece & 3;
  const int dA = 32 * ks + 4 * g, t = 8 * tq + tok;
  const u32x2 xa = *(const u32x2*)(x + (size_t)t * D + dA), xb = *(const u32x2*)(x + (size_t)t * D + dA + 16);
  const u16* rt = (const u16*)(ws + WS_RT) + ((size_t)tq * D + dA) * 8 + tok;
  u16 rv[8];
#pragma unroll
  for (int j = 0; j < 4; ++j) { rv[j] = rt[8 * j]; rv[4 + j] = rt[128 + 8 * j]; }
  const float* mf = (const float*)(ws + WS_MODF);
  const f32x4 sbA = *(const f32x4*)(mf + 3 * D + dA), sbB = *(const f32x4*)(mf + 3 * D + dA + 16);
  const f32x4 cbA = *(const f32x4*)(mf + 4 * D + dA), cbB = *(const f32x4*)(mf + 4 * D + dA + 16);
  u32 p[4];
  p[0] = pk_bf(bflo(xa[0]) + bf2f(rv[0]), bfhi(xa[0]) + bf2f(rv[1]));
  p[1] = pk_bf(bflo(xa[1]) + bf2f(rv[2]), bfhi(xa[1]) + bf2f(rv[3]));
  p[2] = pk_bf(bflo(xb[0]) + bf2f(rv[4]), bfhi(xb[0]) + bf2f(rv[5]));
  p[3] = pk_bf(bflo(xb[1]) + bf2f(rv[6]), bfhi(xb[1]) + bf2f(rv[7]));
  u16* x1 = (u16*)(ws + WS_X1) + (size_t)t * D + dA;
  *(u32x2*)x1 = u32x2{p[0], p[1]};
  *(u32x2*)(x1 + 16) = u32x2{p[2], p[3]};
  float e[8];
#pragma unroll
  for (int j = 0; j < 4; ++j) { e[2 * j] = bflo(p[j]); e[2 * j + 1] = bfhi(p[j]); }
  float s = ((e[0] + e[1]) + (e[2] + e[3])) + ((e[4] + e[5]) + (e[6] + e[7]));
  s = sum_hi3(s);
  if (lane < 8) red[0][wid][lane] = s;
  __syncthreads();
  float mean = 0;
#pragma unroll
  for (int w = 0; w < 9; ++w) mean += red[0][w][tok];
  mean *= (1.0f / D);
  float q = 0;
#pragma unroll
  for (int j = 0; j < 8; ++j) { const float dl = e[j] - mean; q += dl * dl; }
  q = sum_hi3(q);
  if (lane < 8) red[1][wid][lane] = q;
  __syncthreads();
  float var = 0;
#pragma unroll
  for (int w = 0; w < 9; ++w) var += red[1][w][tok];
  var *= (1.0f / D);
  const float m = bfr(mean);
  const float rs = __builtin_amdgcn_rcpf(bfr(__builtin_amdgcn_sqrtf(bfr(bfr(var) + 1e-5f))));
  u32x4 o;
#pragma unroll
  for (int j = 0; j < 4; ++j) {
    const float c0 = j < 2 ? cbA[2 * j] : cbB[2 * j - 4], c1 = j < 2 ? cbA[2 * j + 1] : cbB[2 * j - 3];
    const float s0 = j < 2 ? sbA[2 * j] : sbB[2 * j - 4], s1 = j < 2 ? sbA[2 * j + 1] : sbB[2 * j - 3];
    const u32 dd = pk_bf(e[2 * j] - m, e[2 * j + 1] - m);
    const u32 xh = pk_bf(bflo(dd) * rs, bfhi(dd) * rs);
    const u32 mm = pk_bf(bflo(xh) * c0, bfhi(xh) * c1);
    o[j] = pk_bf(bflo(mm) + s0, bfhi(mm) + s1);
  }
  ((u32x4*)(ws + WS_H2T))[((size_t)(tq >> 1) * 18 + ks) * 64 + 16 * g + 8 * (tq & 1) + tok] = o;
  STAMP_END(2);
}

__device__ __forceinline__ void glds16(const void* base, u32 voff, u32 lds) {
  asm volatile("s_mov_b32 m0, %2\n\ts_nop 0\n\tglobal_load_lds_dwordx4 %0, %1" ::"v"(voff), "s"(base), "s"(lds) : "memory");
}
__device__ __forceinline__ void glds16_q(const void* base, u32 voff, u32 lds) {
  asm volatile("s_mov_b32 m0, %2\n\ts_mov_b64 exec, 0xffff\n\tglobal_load_lds_dwordx4 %0, %1\n\ts_mov_b64 exec, -1" ::"v"(voff), "s"(base), "s"(lds) : "memory");
}
__device__ __forceinline__ int fresh_lane() {
  int l;
  asm volatile("v_mbcnt_lo_u32_b32 %0, -1, 0\n\tv_mbcnt_hi_u32_b32 %0, -1, %0" : "=v"(l));
  return l;
}
__device__ __forceinline__ u32 lds_addr(const void* p) { return (u32)(size_t)(const LAS char*)p; }
__device__ __forceinline__ bf16x8 tr_frag(const char* p, int o2) {
  const s16x4 a = __builtin_amdgcn_ds_read_tr16_b64_v4i16((LAS s16x4*)p);
  const s16x4 b = __builtin_amdgcn_ds_read_tr16_b64_v4i16((LAS s16x4*)(p + o2));
  return __builtin_bit_cast(bf16x8, __builtin_shufflevector(a, b, 0, 1, 2, 3, 4, 5, 6, 7));
}

#define GLU 1
#define CODE_P 7
#define FREE 3
#define GPRE 0
#define K4MAP 0
#define GST 2
__device__ __forceinline__ void st16(char* p, u32x4 v) {
  asm volatile("global_store_dwordx4 %0, %1, off sc1\n\ts_nop 1" ::"v"(p), "v"(v) : "memory");
}
constexpr int G2_STAGE = 64 * 288, G2_LDS = 8 * G2_STAGE;

__device__ __forceinline__ void glds16_h(const void* base, u32 voff, u32 lds) {
  asm volatile("s_mov_b32 m0, %2\n\ts_mov_b32 exec_hi, 0\n\tglobal_load_lds_dwordx4 %0, %1\n\ts_mov_b32 exec_hi, -1" ::"v"(voff), "s"(base), "s"(lds) : "memory");
}
__device__ __forceinline__ constexpr int slab_row(int sg) { return 288 * (sg / 9) + (sg % 9 < 8 ? 72 * ((sg % 9) >> 1) + 32 * ((sg % 9) & 1) : 64); }

constexpr int G1_W1 = 16384, G1_W3 = G1_W1 + 9216 + 16, G1_STAGE = 35072, G1_LDS = 4 * G1_STAGE;

extern "C" __global__ __launch_bounds__(512, 2) void k4_kernel(const u16* __restrict__ w1, const u16* __restrict__ w3, const u16* __restrict__ w2,
                                                               char* __restrict__ ws, u16* __restrict__ out) {
  extern __shared__ __attribute__((aligned(16))) char smem[];
  const int tid = threadIdx.x, lane = tid & 63, wid = __builtin_amdgcn_readfirstlane(tid >> 6);
  STAMP_BEGIN();
  const int mt = (blockIdx.x >> 3) & 7, ft = (blockIdx.x & 7) * 4 + (blockIdx.x >> 6);
  const int wm = wid & 3, wn = wid >> 2;
  const char* tsrc = ws + WS_H2T + (size_t)(16 * mt + 2 * wid) * 18 * 1024;
  const char* wsrc = (const char*)(wn ? w3 : w1) + (size_t)(8 * wm) * (F * 2) + ft * 288;
  const int x1 = 64 + lane, x2 = 128 + (lane & 15);
  const u32 o0 = (lane / 18) * (F * 2) + (lane % 18) * 16, o1 = (x1 / 18) * (F * 2) + (x1 % 18) * 16, o2 = (x2 / 18) * (F * 2) + (x2 % 18) * 16;
  const u32 ol = lane * 16;
  const u32 tdst = lds_addr(smem) + 2 * wid * 1024, wdst = lds_addr(smem) + (wn ? G1_W3 : G1_W1) + wm * 2304;
  auto stage = [&](int p) {
    const u32 sb = (p & 3) * G1_STAGE;
    glds16(tsrc + p * 1024, ol, sb + tdst);
    glds16(tsrc + (18 + p) * 1024, ol, sb + tdst + 1024);
    const char* wp = wsrc + (size_t)p * 32 * (F * 2);
    glds16(wp, o0, sb + wdst);
    glds16(wp, o1, sb + wdst + 1024);
    glds16_q(wp, o2, sb + wdst + 2048);
  };
  const int g = lane >> 4, q = (lane >> 2) & 3, pc = lane & 3;
  const int boff = wm * 4096 + lane * 16;
  const int aoff = G1_W1 + (4 * g + q) * 288 + wn * 144 + 8 * pc;
  const int moff = (pc < 2 ? G1_W1 : G1_W3) + (4 * g + q) * 288 + wn * 144 + 128 + 8 * (pc & 1);
  f32x4 acc[9][4];
#pragma unroll
  for (int i = 0; i < 9; ++i)
#pragma unroll
    for (int j = 0; j < 4; ++j) acc[i][j] = f32x4{0, 0, 0, 0};

  stage(0); stage(1); stage(2);
  auto warm = [&](const char* base, u32 voff) {
    asm volatile("s_mov_b32 m0, %2\n\ts_nop 0\n\tglobal_load_lds_dword %0, %1" ::"v"(voff), "s"(base), "s"(lds_addr(smem) + G1_LDS + wid * 256) : "memory");
  };
  WAIT_V(10); BAR();
  STAMP_MID(0);
  if (wn) BAR();
#pragma unroll
  for (int p = 0; p < 18; ++p) {
    const char* sb = smem + (p & 3) * G1_STAGE;
    bf16x8 A[9], B[4];
#pragma unroll
    for (int j = 0; j < 4; ++j) B[j] = *(const bf16x8*)(sb + boff + j * 1024);
#pragma unroll
    for (int i = 0; i < 4; ++i) {
      A[i] = tr_frag(sb + aoff + 32 * i, 16 * 288);
      A[4 + i] = tr_frag(sb + aoff + (G1_W3 - G1_W1) + 32 * i, 16 * 288);
    }
    A[8] = tr_frag(sb + moff, 16 * 288);
    if (p + 3 < 18) stage(p + 3);
    if (p < 8 && wid == p) {
      const u32 l = fresh_lane();
      if (p < 4) {
        constexpr u32 NL = (D - PF_ROWS) * 3 / 8;
        const u32 i = mt * NL + min((p & 1) * 64 + l, NL - 1);
        warm((const char*)(p < 2 ? w1 : w3) + (size_t)PF_ROWS * (F * 2) + ((ft * 288) & ~127), (i / 3) * (F * 2) + (i % 3) * 128);
      } else if (p < 7) {
        warm((const char*)w2 + (size_t)blockIdx.x * 162 * 128, min((p - 4) * 64 + l, 161u) * 128);
      } else {
        unsigned long long pc;
        u32 len;
        asm volatile("s_getpc_b64 %0\n.Lk4_here:\n\ts_mov_b32 %1, kz_end-.Lk4_here" : "=s"(pc), "=s"(len));
        warm((const char*)pc, min((((blockIdx.x >> 3) & 7) * 64 + l) * 128u, len - 4));
      }
    }
    if (GPRE && p == GPRE) {
      const u32 l = fresh_lane();
      warm(ws + WS_GT + ((size_t)(16 * mt + 4 * wm) * 144 + 9 * (ft >> 1) + 2 * (2 * (ft & 1) + wn)) * 1024, (l >> 4) * 147456 + (l & 15) * 128);
    }
    constexpr int NP = 18 - FREE;
    if (p < NP) {
      if (p < 15) WAIT_V(10); else if (p == 15) WAIT_V(5); else if (p == 16) WAIT_V(0);
      WAIT_L0(); SCHED();
      BAR();
      __builtin_amdgcn_s_setprio(1);
    }
#pragma unroll
    for (int i = 0; i < 9; ++i)
#pragma unroll
      for (int j = 0; j < 4; ++j) acc[i][j] = __builtin_amdgcn_mfma_f32_16x16x32_bf16(A[i], B[j], acc[i][j], 0, 0, 0);
    if (p < NP) __builtin_amdgcn_s_setprio(0);
    SCHED();
    if (p < NP - 1) BAR();
    else if (p == NP - 1) {
      if (wn) WAIT_V(0);
      if (FREE || !wn) BAR();
      if (FREE && !wn) { WAIT_V(0); BAR(); __builtin_amdgcn_s_setprio(2); }
    }
  }
  const int ln = fresh_lane(), g2 = ln >> 4, q2 = (ln >> 2) & 3, pc2 = ln & 3;
  const u32 ol2 = ln * 16;
  const int kq = blockIdx.x & 3, m2 = ((blockIdx.x >> 2) & 1) * 8 + ((blockIdx.x >> 3) & 7), cb = blockIdx.x >> 6;
  const int kg = wn, hs = wm >> 1;
  const char* gsrc = ws + WS_GT + ((size_t)(8 * m2 + 2 * wm) * 144 + 36 * kq + 18 * kg) * 1024;
  const char* w2src = (const char*)w2 + (size_t)(1152 * kq + 576 * kg) * (D * 2) + cb * 288;
  u32 wo[5];
#pragma unroll
  for (int i = 0; i < 5; ++i) { const int x = 64 * i + (i < 4 ? ln : ln & 31); wo[i] = (x / 18) * (D * 2) + (x % 18) * 16; }
  const u32 wo2l = wo[2] + (ln >= 16 ? 64 * (D * 2) : 0);
  const u32 w2dst = lds_addr(smem) + kg * 4 * G2_STAGE + wm * 4608;
  u32x4 B[4][4];
  auto issue = [&](int j, int i) {
    if (i < 5) {
      const bool left = hs ? (2 * j + 1) % 9 == 8 : (2 * j) % 9 == 8;
      const int row = (hs ? slab_row(2 * j + 1) : slab_row(2 * j)) + (left ? 144 : 16) * (wm & 1) + (left && i > 2 ? 64 : 0);
      const char* p = w2src + (size_t)row * (D * 2);
      const u32 dst = w2dst + (j & 3) * G2_STAGE + i * 1024;
      if (i == 4) glds16_h(p, wo[4], dst);
      else if (i == 2 && (j == 4 || j == 8)) glds16(p, left ? wo2l : wo[2], dst);
      else glds16(p, wo[i], dst);
    } else {
      const int f = i - 5;
      asm volatile("global_load_dwordx4 %0, %1, %2 sc1" : "=v"(B[j & 3][f]) : "v"(ol2), "s"(gsrc + ((size_t)(f & 1) * 144 + 2 * j + (f >> 1)) * 1024) : "memory");
    }
  };
  STAMP_MID(1);
  const int P = ft >> 1, qq = 2 * (ft & 1) + wn;
  char* gbase = ws + WS_GT + ((size_t)(16 * mt + 4 * wm) * 144 + 9 * P) * 1024;
  auto glu2 = [&](float g0, float g1, float u0, float u1) -> u32 {
    const f32x2 gv = {g0, g1}, uv = {u0, u1};
    const f32x2 t = gv * -1.4426950408889634f;
    const f32x2 dn = f32x2{__builtin_amdgcn_exp2f(t[0]), __builtin_amdgcn_exp2f(t[1])} + 1.0f;
    const f32x2 o = (gv * uv) * f32x2{__builtin_amdgcn_rcpf(dn[0]), __builtin_amdgcn_rcpf(dn[1])};
    return pk_bf(o[0], o[1]);
  };
#pragma unroll
  for (int j = 0; j < 4; ++j)
#pragma unroll
    for (int s = 0; s < 2; ++s) {
      u32x4 o;
#pragma unroll
      for (int h = 0; h < 2; ++h) {
        const f32x4 a = acc[2 * s + h][j], u = acc[4 + 2 * s + h][j];
        o[2 * h] = glu2(a[0], a[1], u[0], u[1]);
        o[2 * h + 1] = glu2(a[2], a[3], u[2], u[3]);
      }
      st16(gbase + (size_t)j * 144 * 1024 + (2 * qq + s) * 1024 + ln * 16, o);
    }
#pragma unroll
  for (int jp = 0; jp < 2; ++jp) {
    float a[4], u[4];
#pragma unroll
    for (int e = 0; e < 4; ++e) {
      const float m0 = acc[8][2 * jp][e], m1 = acc[8][2 * jp + 1][e];
      auto r = __builtin_amdgcn_permlane32_swap(__builtin_bit_cast(u32, m0), __builtin_bit_cast(u32, m1), false, false);
      a[e] = __builtin_bit_cast(float, (u32)r[0]); u[e] = __builtin_bit_cast(float, (u32)r[1]);
    }
    const u32x2 o = {glu2(a[0], a[1], u[0], u[1]), glu2(a[2], a[3], u[2], u[3])};
    const int j = 2 * jp + (ln >> 5);
    char* gp = gbase + (size_t)j * 144 * 1024 + 8 * 1024 + (16 * (2 * (qq & 1) + (g2 & 1)) + (ln & 15)) * 16 + 8 * (qq >> 1);
    asm volatile("global_store_dwordx2 %0, %1, off sc1\n\ts_nop 1" ::"v"(gp), "v"(o) : "memory");
  }
  __builtin_amdgcn_s_setprio(0);
  WAIT_V(0); BAR();
  STAMP_MID(2);
  {
    u32* gfl = (u32*)(ws + WS_FLAG) + 8192;
    if (wid == 0) {
      if (ln == 0) __hip_atomic_store(gfl + (mt * 4 + (ft >> 3)) * 32 + (ft & 7), 1u, __ATOMIC_RELAXED, __HIP_MEMORY_SCOPE_AGENT);
    }
#pragma unroll
    for (int j = 0; j < 2; ++j)
#pragma unroll
      for (int i = 0; i < 5; ++i) issue(j, i);
    if (wid == 1) {
#pragma unroll 1
      for (int spin = 0; spin < (1 << 20); ++spin) {
        u32 f;
        asm volatile("global_load_dword %0, %1, off sc1\n\ts_waitcnt vmcnt(0)" : "=v"(f) : "v"(gfl + ((m2 >> 1) * 4 + kq) * 32 + (ln & 7)) : "memory");
        if (__builtin_amdgcn_ballot_w64(f != 1u) == 0) break;
      }
    }
    BAR();
  }
#define WAIT_B(n, j) asm volatile("s_waitcnt vmcnt(" #n ")" : "+v"(B[(j) & 3][0]), "+v"(B[(j) & 3][1]), "+v"(B[(j) & 3][2]), "+v"(B[(j) & 3][3])::"memory")
  const char* abase = smem + kg * 4 * G2_STAGE + (4 * g2 + q2) * 288 + 8 * pc2;
  f32x4 ac2[9][2];
#pragma unroll
  for (int c = 0; c < 9; ++c) ac2[c][0] = ac2[c][1] = f32x4{0, 0, 0, 0};
#pragma unroll
  for (int j = 0; j < 2; ++j)
#pragma unroll
    for (int i = 5; i < 9; ++i) issue(j, i);
  WAIT_B(4, 0); BAR();
  STAMP_MID(3);
  if (kg) BAR();
#pragma unroll
  for (int j = 0; j < 9; ++j) {
    bf16x8 A[2][9];
#pragma unroll
    for (int h = 0; h < 2; ++h)
#pragma unroll
      for (int c = 0; c < 9; ++c) A[h][c] = tr_frag(abase + (j & 3) * G2_STAGE + h * 32 * 288 + 32 * c, 16 * 288);
    WAIT_L0(); SCHED();
    BAR();
    __builtin_amdgcn_s_setprio(1);
#pragma unroll
    for (int i = 0; i < 9; ++i) {
#pragma unroll
      for (int e = 0; e < 2; ++e) {
        const int h = (2 * i + e) / 9, c = (2 * i + e) % 9;
#pragma unroll
        for (int t = 0; t < 2; ++t) {
          const u32x4 bv = B[j & 3][2 * h + t];
          ac2[c][t] = __builtin_amdgcn_mfma_f32_16x16x32_bf16(A[h][c], __builtin_bit_cast(bf16x8, bv), ac2[c][t], 0, 0, 0);
        }
      }
      SCHED();
      if (j + 2 < 9) issue(j + 2, i);
      SCHED();
    }
    __builtin_amdgcn_s_setprio(0);
    if (j < 7) WAIT_B(9, j + 1); else if (j == 7) WAIT_B(0, j + 1);
    SCHED();
    if (j < 8 || !kg) BAR();
  }
  STAMP_MID(4);
  const int et = kg, ep = wm;
  const size_t orow = (size_t)(128 * m2 + 32 * kq + 16 * et + (ln & 15)) * D + 144 * cb + 4 * g2;
  f32x4 gb[3]; u32x2 xr[3];
#pragma unroll
  for (int c = 0; c < 3; ++c) {
    const int ct = c < 2 ? 2 * ep + c : 8;
    gb[c] = *(const f32x4*)((const float*)(ws + WS_MODF) + 5 * D + 144 * cb + 16 * ct + 4 * g2);
    xr[c] = *(const u32x2*)((const u16*)(ws + WS_X1) + orow + 16 * ct);
  }
  f32x4* red = (f32x4*)smem;
#pragma unroll
  for (int c = 0; c < 9; ++c) red[(wid * 9 + c) * 64 + ln] = kg ? ac2[c][0] : ac2[c][1];
  WAIT_L0(); BAR();
  const int tile = m2 * 4 + cb;
  char* xch = ws + WS_XCH + (size_t)tile * (12 * 9216);
  f32x4* own = red + 72 * 64;
  {
    f32x4 v[9];
#pragma unroll
    for (int c = 0; c < 9; ++c) v[c] = (kg ? ac2[c][1] : ac2[c][0]) + red[((wid ^ 4) * 9 + c) * 64 + ln];
    if (wm == kq) {
#pragma unroll
      for (int c = 0; c < 9; ++c) own[(kg * 9 + c) * 64 + ln] = v[c];
    } else {
      char* p = xch + ((wm * 3 + (kq > wm ? kq - 1 : kq)) * 2 + kg) * 4608;
#pragma unroll
      for (int c = 0; c < 4; ++c) {
        const u32x4 w = {pk_bf(v[2 * c][0], v[2 * c][1]), pk_bf(v[2 * c][2], v[2 * c][3]), pk_bf(v[2 * c + 1][0], v[2 * c + 1][1]),
                         pk_bf(v[2 * c + 1][2], v[2 * c + 1][3])};
        asm volatile("global_store_dwordx4 %0, %1, off sc1\n\ts_nop 1" ::"v"(p + c * 1024 + ln * 16), "v"(w) : "memory");
      }
      const u32x2 w = {pk_bf(v[8][0], v[8][1]), pk_bf(v[8][2], v[8][3])};
      asm volatile("global_store_dwordx2 %0, %1, off sc1\n\ts_nop 1" ::"v"(p + 4096 + ln * 8), "v"(w) : "memory");
    }
  }
  asm volatile("s_waitcnt vmcnt(0) lgkmcnt(0)" : "+v"(gb[0]), "+v"(gb[1]), "+v"(gb[2]), "+v"(xr[0]), "+v"(xr[1]), "+v"(xr[2])::"memory");
  BAR();
  u32* flags = (u32*)(ws + WS_FLAG) + tile * 128;
  if (wid == 0) {
    if (ln == 0) {
#pragma unroll
      for (int r = 0; r < 4; ++r)
        if (r != kq) __hip_atomic_store(flags + r * 32 + (kq > r ? kq - 1 : kq), 1u, __ATOMIC_RELAXED, __HIP_MEMORY_SCOPE_AGENT);
    }
  } else if (wid == 1) {
#pragma unroll 1
    for (int spin = 0; spin < (1 << 20); ++spin) {
      u32x4 f;
      asm volatile("global_load_dwordx4 %0, %1, off sc1\n\ts_waitcnt vmcnt(0)" : "=v"(f) : "v"(flags + kq * 32) : "memory");
      if (__builtin_amdgcn_readfirstlane(f[0] & f[1] & f[2]) == 1u) break;
    }
  }
  BAR();
  STAMP_MID(5);
  {
    u32x4 pr[3]; u32x2 pl[3];
    const char* src = xch + ((size_t)kq * 6 + et) * 4608;
    const char *s4 = src + ep * 1024 + ln * 16, *s2 = src + 4096 + ln * 8;
    asm volatile("global_load_dwordx4 %0, %6, off sc1\n\tglobal_load_dwordx4 %1, %7, off sc1\n\tglobal_load_dwordx4 %2, %8, off sc1\n\t"
                 "global_load_dwordx2 %3, %9, off sc1\n\tglobal_load_dwordx2 %4, %10, off sc1\n\tglobal_load_dwordx2 %5, %11, off sc1\n\t"
                 "s_waitcnt vmcnt(0)"
                 : "=&v"(pr[0]), "=&v"(pr[1]), "=&v"(pr[2]), "=&v"(pl[0]), "=&v"(pl[1]), "=&v"(pl[2])
                 : "v"(s4), "v"(s4 + 9216), "v"(s4 + 18432), "v"(s2), "v"(s2 + 9216), "v"(s2 + 18432) : "memory");
#pragma unroll
    for (int c = 0; c < 3; ++c) {
      const int ct = c < 2 ? 2 * ep + c : 8;
      f32x4 s = own[(et * 9 + ct) * 64 + ln];
#pragma unroll
      for (int k = 0; k < 3; ++k) {
        const u32 lo = c < 2 ? pr[k][2 * c] : pl[k][0], hi = c < 2 ? pr[k][2 * c + 1] : pl[k][1];
        s += f32x4{bflo(lo), bfhi(lo), bflo(hi), bfhi(hi)};
      }
      u32x2 o;
#pragma unroll
      for (int h = 0; h < 2; ++h) {
        const u32 y = pk_bf(s[2 * h], s[2 * h + 1]);
        const u32 z = pk_bf(gb[c][2 * h] * bflo(y), gb[c][2 * h + 1] * bfhi(y));
        o[h] = pk_bf(bflo(xr[c][h]) + bflo(z), bfhi(xr[c][h]) + bfhi(z));
      }
      if (c < 2 || ep == 0) *(u32x2*)(out + orow + 16 * ct) = o;
    }
  }
  STAMP_END(3);
}

extern "C" __device__ __attribute__((used, noinline)) void kz_end() {}

extern "C" void kernel_launch(void* const* d_in, const int* in_sizes, int n_in, void* d_out, int out_size, void* d_ws, size_t ws_size,
                              hipStream_t stream) {
  const u16* u = (const u16*)d_in[0];
  char* ws = (char*)d_ws;
  (void)hipFuncSetAttribute((const void*)k4_kernel, hipFuncAttributeMaxDynamicSharedMemorySize, G2_LDS);
  k1_kernel<<<K1_MOD + K1_LN + K1_KG, 256, 0, stream>>>(u, (const u16*)d_in[1], (const u16*)d_in[2], (const u16*)d_in[3], (const u16*)d_in[4],
                                                        (const u16*)d_in[5], (const u16*)d_in[6], (const u16*)d_in[7], (const u16*)d_in[13],
                                                        (const u16*)d_in[14], ws);
  k2_kernel<<<D, 256, 0, stream>>>((const u16*)d_in[8], (const u16*)d_in[10], (const u16*)d_in[11], ws);
  k3_kernel<<<256, 576, 0, stream>>>(u, ws);
  k4_kernel<<<256, 512, G2_LDS, stream>>>((const u16*)d_in[10], (const u16*)d_in[11], (const u16*)d_in[12], ws, (u16*)d_out);
}
```

```cpp
#include <hip/hip_runtime.h>
#include <hip/hip_bf16.h>

typedef unsigned short u16;
typedef unsigned int u32;
typedef __attribute__((ext_vector_type(2))) float f32x2;
typedef __attribute__((ext_vector_type(4))) float f32x4;
typedef __attribute__((ext_vector_type(2))) u32 u32x2;
typedef __attribute__((ext_vector_type(4))) u32 u32x4;
typedef __attribute__((ext_vector_type(4))) short s16x4;
typedef __attribute__((ext_vector_type(8))) short s16x8;
typedef __attribute__((ext_vector_type(2))) __bf16 bf16x2;
typedef __attribute__((ext_vector_type(8))) __bf16 bf16x8;
typedef __attribute__((ext_vector_type(2))) _Float16 f16x2;
typedef __attribute__((ext_vector_type(8))) _Float16 f16x8;
#define LAS __attribute__((address_space(3)))
#define GAS __attribute__((address_space(1)))

constexpr int L = 2048, D = 576, NM = 128, F = 4608, MODN = 6 * D;
constexpr int KSPLIT = 3;

constexpr size_t WS_MODP = 0;
constexpr size_t WS_MODF = 65536;
constexpr size_t WS_XHT  = 131072;
constexpr size_t ACT_B   = (size_t)L * D * 2;
constexpr size_t WS_KX   = WS_XHT + ACT_B;
constexpr size_t WS_RT   = WS_KX + ACT_B;
constexpr size_t WS_X1   = WS_RT + ACT_B;
constexpr size_t WS_H2T  = WS_X1 + ACT_B;
constexpr size_t WS_GT   = WS_XHT + 5 * ACT_B;
#define XOVER 0
constexpr size_t WS_XCH  = XOVER ? WS_XHT : WS_GT + (size_t)L * F * 2;
constexpr size_t WS_FLAG = 81920;

#define WAIT_V(n) asm volatile("s_waitcnt vmcnt(" #n ")" ::: "memory")
#define WAIT_L0() asm volatile("s_waitcnt lgkmcnt(0)" ::: "memory")
#define BAR() __builtin_amdgcn_s_barrier()
#define SCHED() __builtin_amdgcn_sched_barrier(0)
#define STAMP_BEGIN()
#define STAMP_MID(i)
#define STAMP_END(k)
__device__ __forceinline__ float bf2f(u16 v) { return __builtin_bit_cast(float, (u32)v << 16); }
__device__ __forceinline__ float bflo(u32 v) { return __builtin_bit_cast(float, v << 16); }
__device__ __forceinline__ float bfhi(u32 v) { return __builtin_bit_cast(float, v & 0xffff0000u); }
__device__ __forceinline__ u32 pk_bf(float a, float b) { f32x2 v = {a, b}; return __builtin_bit_cast(u32, __builtin_convertvector(v, bf16x2)); }
__device__ __forceinline__ u32 pk_h(float a, float b) { f32x2 v = {a, b}; return __builtin_bit_cast(u32, __builtin_convertvector(v, f16x2)); }
__device__ __forceinline__ float bfr(float a) { return bflo(pk_bf(a, a)); }

template <int CTRL> __device__ __forceinline__ float dpp(float v) {
  return __builtin_bit_cast(float, __builtin_amdgcn_update_dpp(0, __builtin_bit_cast(int, v), CTRL, 0xf, 0xf, true));
}
__device__ __forceinline__ float sum16x(float v) {
  u32 u = __builtin_bit_cast(u32, v);
  auto r = __builtin_amdgcn_permlane16_swap(u, u, false, false);
  return __builtin_bit_cast(float, (u32)r[0]) + __builtin_bit_cast(float, (u32)r[1]);
}
__device__ __forceinline__ float sum32x(float v) {
  u32 u = __builtin_bit_cast(u32, v);
  auto r = __builtin_amdgcn_permlane32_swap(u, u, false, false);
  return __builtin_bit_cast(float, (u32)r[0]) + __builtin_bit_cast(float, (u32)r[1]);
}
__device__ __forceinline__ float wave_sum(float v) {
  v += dpp<0xB1>(v);
  v += dpp<0x4E>(v);
  v += dpp<0x141>(v);
  v += dpp<0x140>(v);
  return sum32x(sum16x(v));
}
__device__ __forceinline__ float sum_hi3(float v) {
  v += dpp<0x128>(v);
  return sum32x(sum16x(v));
}
__device__ __forceinline__ float silu_f(float g) { return g * __builtin_amdgcn_rcpf(1.0f + __builtin_amdgcn_exp2f(-1.4426950408889634f * g)); }

__device__ __forceinline__ u32 touch(const void* p) {
  u32 r;
  asm volatile("global_load_dword %0, %1, off" : "=v"(r) : "v"(p) : "memory");
  return r;
}
#define TOUCH_DONE(r) asm volatile("s_waitcnt vmcnt(0)" : "+v"(r)::"memory")

#define K1ORD 0
constexpr int K1_MOD = 54 * KSPLIT, K1_LN = 256, K1_KG = D;
constexpr int TROW = 528;

typedef f32x2 cplx;
__device__ __forceinline__ cplx cmul(cplx a, cplx z) {
  cplx t, r;
  asm("v_pk_mul_f32 %0, %1, %2 op_sel_hi:[1,0]" : "=v"(t) : "v"(a), "v"(z));
  asm("v_pk_fma_f32 %0, %1, %2, %3 op_sel:[1,1,0] op_sel_hi:[0,1,1] neg_lo:[1,0,0]" : "=v"(r) : "v"(a), "v"(z), "v"(t));
  return r;
}
__device__ __forceinline__ cplx cexpl(float al2, float brev, float l) {
  const float m = __builtin_amdgcn_exp2f(al2 * l);
  const float ph = __builtin_amdgcn_fractf(brev * l);
  return cplx{m * __builtin_amdgcn_cosf(ph), m * __builtin_amdgcn_sinf(ph)};
}

extern "C" __global__ __launch_bounds__(256) void k1_kernel(const u16* __restrict__ x, const u16* __restrict__ Are, const u16* __restrict__ Aim,
                                                 const u16* __restrict__ Bre, const u16* __restrict__ Bim, const u16* __restrict__ Cre,
                                                 const u16* __restrict__ Cim, const u16* __restrict__ logdt, const u16* __restrict__ cvec,
                                                 const u16* __restrict__ wmod, char* __restrict__ ws) {
  __shared__ __attribute__((aligned(16))) char smem[64 * TROW + 2624];
  STAMP_BEGIN();
  const int tid = threadIdx.x, lane = tid & 63, wid = tid >> 6;
  int b = blockIdx.x;
  u32 pf = 0;
  if (b < 16) {
    unsigned long long pc;
    u32 len;
    asm volatile("s_getpc_b64 %0\n.Lk1_here:\n\ts_mov_b32 %1, kz_end-.Lk1_here" : "=s"(pc), "=s"(len));
    const u32 o = ((b >> 3) * 256 + tid) * 128;
    if (o < len) pf = touch((const void*)(pc + o));
  }
#define K1_DONE() TOUCH_DONE(pf)
  b = b < K1_KG ? b + K1_MOD + K1_LN : b - K1_KG;
  if (b < K1_MOD) {
    const int cb = b % 54, kq = b / 54, cc = tid & 7, rg = tid >> 3;
    u32x4 w[6]; u16 cv[6];
#pragma unroll
    for (int i = 0; i < 6; ++i) {
      const int row = 192 * kq + rg + 32 * i;
      w[i] = *(const u32x4*)(wmod + (size_t)row * MODN + 64 * cb + 8 * cc);
      cv[i] = cvec[row];
    }
    float acc[8] = {0, 0, 0, 0, 0, 0, 0, 0};
#pragma unroll
    for (int i = 0; i < 6; ++i) {
      const float s = bfr(silu_f(bf2f(cv[i])));
#pragma unroll
      for (int j = 0; j < 4; ++j) { acc[2 * j] += s * bflo(w[i][j]); acc[2 * j + 1] += s * bfhi(w[i][j]); }
    }
#pragma unroll
    for (int j = 0; j < 8; ++j) acc[j] = sum_hi3(acc[j]);
    float* red = (float*)smem;
    if (lane < 8) {
      *(f32x4*)(red + wid * 64 + lane * 8) = f32x4{acc[0], acc[1], acc[2], acc[3]};
      *(f32x4*)(red + wid * 64 + lane * 8 + 4) = f32x4{acc[4], acc[5], acc[6], acc[7]};
    }
    __syncthreads();
    if (tid < 64) ((float*)(ws + WS_MODP))[((cb % 9) * 64 + tid) * 24 + (cb / 9) * 4 + kq] = (red[tid] + red[64 + tid]) + (red[128 + tid] + red[192 + tid]);
    STAMP_END(0);
    return;
  }
  b -= K1_MOD;
  if (b < K1_LN) {
    u32 v[2][4]; u16 t[2];
#pragma unroll
    for (int r = 0; r < 2; ++r) {
      const u16* row = x + (size_t)(8 * b + 2 * wid + r) * D;
#pragma unroll
      for (int i = 0; i < 4; ++i) v[r][i] = *(const u32*)(row + 2 * (lane + 64 * i));
      t[r] = row[512 + lane];
    }
    u32 o[2][5];
#pragma unroll
    for (int r = 0; r < 2; ++r) {
      float e[9];
#pragma unroll
      for (int i = 0; i < 4; ++i) { e[2 * i] = bflo(v[r][i]); e[2 * i + 1] = bfhi(v[r][i]); }
      e[8] = bf2f(t[r]);
      float s = 0;
#pragma unroll
      for (int i = 0; i < 9; ++i) s += e[i];
      const float mean = wave_sum(s) * (1.0f / D);
      float q = 0;
#pragma unroll
      for (int i = 0; i < 9; ++i) { const float dlt = e[i] - mean; q += dlt * dlt; }
      const float var = wave_sum(q) * (1.0f / D);
      const float m = bfr(mean);
      const float rs = __builtin_amdgcn_rcpf(bfr(__builtin_amdgcn_sqrtf(bfr(bfr(var) + 1e-5f))));
#pragma unroll
      for (int i = 0; i < 4; ++i) {
        const u32 dd = pk_bf(e[2 * i] - m, e[2 * i + 1] - m);
        o[r][i] = pk_bf(bflo(dd) * rs, bfhi(dd) * rs);
      }
      o[r][4] = pk_bf(bfr(e[8] - m) * rs, 0.f);
    }
    u32* tile = (u32*)smem;
#pragma unroll
    for (int i = 0; i < 4; ++i) {
      tile[(2 * (lane + 64 * i)) * 4 + wid] = __builtin_amdgcn_perm(o[1][i], o[0][i], 0x05040100);
      tile[(2 * (lane + 64 * i) + 1) * 4 + wid] = __builtin_amdgcn_perm(o[1][i], o[0][i], 0x07060302);
    }
    tile[(512 + lane) * 4 + wid] = __builtin_amdgcn_perm(o[1][4], o[0][4], 0x05040100);
    __syncthreads();
    u32x4* dst = (u32x4*)(ws + WS_XHT) + (size_t)b * D;
#pragma unroll
    for (int i = 0; i < 3; ++i) {
      const int c = tid + 256 * i;
      if (c < D) dst[c] = *(const u32x4*)(tile + c * 4);
    }
    STAMP_END(0);
    return;
  }
  b -= K1_LN;
  {
    const int n = tid & 127, half = __builtin_amdgcn_readfirstlane(tid >> 7);
    const int idx = b * NM + n;
    const float are = bf2f(Are[idx]), aim = bf2f(Aim[idx]), bre = bf2f(Bre[idx]), bim = bf2f(Bim[idx]);
    const float cre = bf2f(Cre[idx]), cim = bf2f(Cim[idx]);
    const float dt = bfr(__builtin_amdgcn_exp2f(1.4426950408889634f * bf2f(logdt[b])));
    const float al2 = (dt * are) * 1.4426950408889634f, brev = (dt * aim) * 0.15915494309189535f;
    cplx c = cmul(cplx{cre, cim}, cplx{dt * bre, dt * bim});
    const int he = (__builtin_amdgcn_frexp_expf(fmaxf(fabsf(c[0]), fabsf(c[1])))) >> 1;
    c = cplx{__builtin_amdgcn_ldexpf(c[0], -he), __builtin_amdgcn_ldexpf(c[1], -he)};
    char* Ut = smem + n * 4;
#define PUT(row, z) (*(u32*)(Ut + (row) * TROW) = pk_h((z)[0], (z)[1]))
    const cplx z128 = cexpl(al2, brev, 128.f);
    if (half == 0) {
      const cplx z8c = cexpl(al2, -brev, 8.f);
      cplx v = {__builtin_amdgcn_ldexpf(1.0f, he), 0.f};
      cplx u = cmul(c, cexpl(al2, brev, 1024.f));
#pragma unroll
      for (int j = 0; j < 16; ++j) {
        PUT(48 + j, v); if (j < 15) v = cmul(v, z8c);
        if (j < 9) { PUT(32 + j, u); if (j < 8) u = cmul(u, z128); }
      }
      const cplx bd = {fabsf(c[0]) + fabsf(c[1]), 0.f};
      PUT(41, bd);
    } else {
      const cplx z1 = cexpl(al2, brev, 1.f);
      cplx u[8];
      u[0] = c;
#pragma unroll
      for (int j = 1; j < 8; ++j) u[j] = cmul(u[j - 1], z1);
#pragma unroll
      for (int j = 0; j < 8; ++j) { PUT(j, u[j]); u[j] = cmul(u[j], z128); PUT(8 + j, u[j]); }
#pragma unroll
      for (int s = 0; s < 2; ++s)
#pragma unroll
        for (int j = 0; j < 4; ++j) { u[2 * j] = cmul(u[2 * j], z128); PUT(16 + 4 * s + j, u[2 * j]); }
#pragma unroll
      for (int s = 0; s < 4; ++s)
#pragma unroll
        for (int j = 0; j < 2; ++j) { u[4 * j] = cmul(u[4 * j], z128); PUT(24 + 2 * s + j, u[4 * j]); }
    }
#undef PUT
    WAIT_L0(); BAR();
    float* Kc = (float*)(smem + 64 * TROW);
    const int wu = __builtin_amdgcn_readfirstlane(wid);
    if (wu < 3) {
      const int r = lane & 15, g = lane >> 4;
      f32x4 acc = {0, 0, 0, 0};
#pragma unroll
      for (int ks = 0; ks < 8; ++ks) {
        const f16x8 a = *(const f16x8*)(smem + (16 * wu + r) * TROW + ks * 64 + g * 16);
        const f16x8 v = *(const f16x8*)(smem + (48 + r) * TROW + ks * 64 + g * 16);
        acc = __builtin_amdgcn_mfma_f32_16x16x32_f16(a, v, acc, 0, 0, 0);
      }
      int o0, sA = 1, sB;
      if (wu == 0) { o0 = (g < 2 ? 4 * g : 120 + 4 * g) + 8 * r; sB = 2; }
      else if (wu == 1) { o0 = g < 2 ? 256 + 64 * g + 4 * r : 256 + 64 * g + 2 * r; sB = g < 2 ? 2 : 32; }
      else { o0 = g < 2 ? 512 + 64 * g + r : (lane == 32 ? 640 : 648); sA = g < 2 ? 16 : 1; sB = g < 2 ? 32 : 4; }
      Kc[o0] = acc[0]; Kc[o0 + sA] = acc[1]; Kc[o0 + sB] = acc[2]; Kc[o0 + sA + sB] = acc[3];
    }
    WAIT_L0(); BAR();
    {
      const int q = tid;
      float k[8];
      if (q < 32) {
#pragma unroll
        for (int e = 0; e < 8; ++e) k[e] = Kc[8 * q + e];
      } else if (q < 64) {
        const float* p = Kc + 256 + 4 * (q - 32);
        k[0] = k[1] = p[0]; k[2] = p[1]; k[3] = k[4] = k[5] = p[2]; k[6] = p[3]; k[7] = p[4];
      } else if (q < 128) {
        const float* p = Kc + 384 + 2 * (q - 64);
        k[0] = k[1] = k[2] = p[0]; k[3] = k[4] = k[5] = p[1]; k[6] = k[7] = p[2];
      } else {
        const float* p = Kc + 512 + (q - 128);
        k[0] = k[1] = k[2] = k[3] = p[0]; k[5] = k[6] = k[7] = p[1]; k[4] = (q & 1) ? p[1] : p[0];
      }
      const int se = 14 - __builtin_amdgcn_frexp_expf(Kc[641]);
      u32x4 o;
#pragma unroll
      for (int e = 0; e < 4; ++e) o[e] = pk_h(__builtin_amdgcn_ldexpf(k[7 - 2 * e], se), __builtin_amdgcn_ldexpf(k[6 - 2 * e], se));
      ((u32x4*)(ws + WS_KX))[(size_t)b * 256 + q] = o;
      if (tid == 0) ((int*)(ws + WS_MODP))[b * 24 + 3] = se;
    }
    K1_DONE();
    STAMP_END(0);
  }
}

#define PF2 288
#define PF3 0
#define PF_ROWS (PF2 + PF3)
#define K3WT 0
constexpr int KCP = 4160;
constexpr int K2_H = 4 * KCP;
constexpr int K2_RED = K2_H + 4608;

extern "C" __global__ __launch_bounds__(256) void k2_kernel(const u16* __restrict__ dskip, const u16* __restrict__ w1, const u16* __restrict__ w3,
                                                            char* __restrict__ ws) {
  __shared__ __attribute__((aligned(16))) char smem[K2_RED + 24 * 1024];
  STAMP_BEGIN();
  const int tid = threadIdx.x, lane = tid & 63, wid = __builtin_amdgcn_readfirstlane(tid >> 6);
  const int d = (blockIdx.x & 7) * 72 + (blockIdx.x >> 3);
  const u32x4* kx = (const u32x4*)(ws + WS_KX) + (size_t)d * 256;
  const u32x4 xh = ((const u32x4*)(ws + WS_XHT))[(size_t)tid * D + d];
  const u32x4 kA = kx[tid];
  u32x2 kB = *(const u32x2*)(kx + max(tid - 1, 0));
  const f32x4* mp = (const f32x4*)(ws + WS_MODP) + d * 6;
  float md[6];
  int se = 0;
#pragma unroll
  for (int i = 0; i < 6; ++i) {
    const f32x4 v = mp[i];
    md[i] = bfr(v[0] + v[1] + v[2]);
    if (i == 0) { const float e = v[3]; se = __builtin_bit_cast(int, e); }
  }
  md[1] = bfr(1.0f + md[1]); md[4] = bfr(1.0f + md[4]);
  const u32 dkw = ((const u32*)dskip)[d >> 1];
  const float dk = (d & 1) ? bfhi(dkw) : bflo(dkw);
  const float sa = md[0], c1 = md[1], ga = md[2];
  const int he = 14 - __builtin_amdgcn_frexp_expf(24.5f * fabsf(c1) + fabsf(sa));
  if (tid == 0) kB = u32x2{0, 0};
  {
    char* p = smem + (255 - tid) * 16;
    const u32 e0 = __builtin_amdgcn_alignbit(kA[1], kA[0], 16), e1 = __builtin_amdgcn_alignbit(kA[2], kA[1], 16);
    const u32 e2 = __builtin_amdgcn_alignbit(kA[3], kA[2], 16), e3 = __builtin_amdgcn_alignbit(kB[0], kA[3], 16);
    const u32 e4 = __builtin_amdgcn_alignbit(kB[1], kB[0], 16);
    *(u32x4*)(p + 0 * KCP) = u32x4{e1, e2, e3, e4};
    *(u32x4*)(p + 1 * KCP) = u32x4{kA[1], kA[2], kA[3], kB[0]};
    *(u32x4*)(p + 2 * KCP) = u32x4{e0, e1, e2, e3};
    *(u32x4*)(p + 3 * KCP) = kA;
    if (tid < 8) {
      *(u32x4*)(smem + (tid & 3) * KCP + (256 + (tid >> 2)) * 16) = u32x4{0, 0, 0, 0};
    }
    u32x4 hh;
#pragma unroll
    for (int i = 0; i < 4; ++i) {
      const u32 m = pk_bf(bflo(xh[i]) * c1, bfhi(xh[i]) * c1);
      const u32 h = pk_bf(bflo(m) + sa, bfhi(m) + sa);
      hh[i] = pk_h(__builtin_amdgcn_ldexpf(bflo(h), he), __builtin_amdgcn_ldexpf(bfhi(h), he));
    }
    *(u32x4*)(smem + K2_H + 512 + tid * 16) = hh;
    if (tid < 32) *(u32x4*)(smem + K2_H + tid * 16) = u32x4{0, 0, 0, 0};
  }
  u32 pf = 0;
  {
    const int i = blockIdx.x * 256 + tid;
    if (i < 2 * PF2 * 72) pf = touch((const char*)(i < PF2 * 72 ? w1 : w3) + (size_t)(i < PF2 * 72 ? i : i - PF2 * 72) * 128);
  }
  STAMP_MID(0);
  WAIT_L0(); BAR();
  STAMP_MID(1);
  if (tid == 0) {
    float* mf = (float*)(ws + WS_MODF);
#pragma unroll
    for (int i = 3; i < 6; ++i) mf[i * D + d] = md[i];
  }
  const int r = lane & 15, g = lane >> 4;
  f32x4 acc[8];
#pragma unroll
  for (int i = 0; i < 8; ++i) acc[i] = f32x4{0, 0, 0, 0};
  const char* abase = smem + (r & 3) * KCP + 2 * (2044 + 8 - 8 * g - (r & 12));
  const char* bbase = smem + K2_H + 512 + 2 * (16 * r - 8 * g + 8);
#pragma unroll
  for (int cls = 0; cls < 2; ++cls) {
    const int i0 = 2 * wid + cls;
    const char* ap = abase - 64 * (7 - i0);
    const char* bp = bbase + 64 * (i0 - 7);
    f16x8 A[8], B[8];
#pragma unroll
    for (int m = 0; m < 8; ++m) {
      const u32x2 lo = *(const volatile LAS u32x2*)(ap - 512 * m), hi = *(const volatile LAS u32x2*)(ap - 512 * m + 8);
      A[m] = __builtin_bit_cast(f16x8, u32x4{lo[0], lo[1], hi[0], hi[1]});
      B[m] = *(const f16x8*)(bp + 512 * m);
    }
#pragma unroll
    for (int n = 0; n < 8; ++n)
#pragma unroll
      for (int k = 0; k <= n; ++k) acc[n] = __builtin_amdgcn_mfma_f32_16x16x32_f16(A[n - k], B[k], acc[n], 0, 0, 0);
  }
  STAMP_MID(2);
  f32x4* red = (f32x4*)(smem + K2_RED);
#pragma unroll
  for (int n = 0; n < 8; ++n) {
    const int o = n >> 1;
    if (o != wid) red[(n * 3 + (wid > o ? wid - 1 : wid)) * 64 + lane] = acc[n];
  }
  WAIT_L0(); BAR();
  STAMP_MID(3);
#pragma unroll
  for (int e = 0; e < 2; ++e) {
    const int n = 2 * wid + e;
    f32x4 s = wid == 0 ? acc[e] : wid == 1 ? acc[2 + e] : wid == 2 ? acc[4 + e] : acc[6 + e];
#pragma unroll
    for (int j = 0; j < 3; ++j) s += red[(n * 3 + j) * 64 + lane];
    const int t = 256 * n + 16 * r + 4 * g;
    const u32x2 hv = *(const u32x2*)(smem + K2_H + 512 + 2 * t);
    u32x2 o;
#pragma unroll
    for (int j = 0; j < 2; ++j) {
      const u32 hw = hv[j];
      const f16x2 hp = __builtin_bit_cast(f16x2, hw);
      const u32 y = pk_bf(__builtin_amdgcn_ldexpf(s[2 * j], -se - he), __builtin_amdgcn_ldexpf(s[2 * j + 1], -se - he));
      const u32 sk = pk_bf(__builtin_amdgcn_ldexpf((float)hp[0], -he) * dk, __builtin_amdgcn_ldexpf((float)hp[1], -he) * dk);
      const u32 ys = pk_bf(bflo(y) + bflo(sk), bfhi(y) + bfhi(sk));
      o[j] = pk_bf(ga * bflo(ys), ga * bfhi(ys));
    }
    *(u32x2*)(ws + WS_RT + ((size_t)(t >> 3) * D + d) * 16 + (t & 4) * 2) = o;
  }
  TOUCH_DONE(pf);
  STAMP_END(1);
}

extern "C" __global__ __launch_bounds__(576) void k3_kernel(const u16* __restrict__ x, const u16* __restrict__ w1, const u16* __restrict__ w3,
                                                            char* __restrict__ ws) {
  __shared__ float red[2][9][8];
  STAMP_BEGIN();
  const int tid = threadIdx.x, lane = tid & 63, wid = tid >> 6;
  const int tq = (blockIdx.x & 7) * 32 + (blockIdx.x >> 3);
  if (tid < 36) ((u32*)(ws + WS_FLAG))[blockIdx.x * 36 + tid] = 0;
  const int tok = lane & 7, piece = tid >> 3, ks = piece >> 2, g = piece & 3;
  const int dA = 32 * ks + 4 * g, t = 8 * tq + tok;
  const u32x2 xa = *(const u32x2*)(x + (size_t)t * D + dA), xb = *(const u32x2*)(x + (size_t)t * D + dA + 16);
  const u16* rt = (const u16*)(ws + WS_RT) + ((size_t)tq * D + dA) * 8 + tok;
  u16 rv[8];
#pragma unroll
  for (int j = 0; j < 4; ++j) { rv[j] = rt[8 * j]; rv[4 + j] = rt[128 + 8 * j]; }
  const float* mf = (const float*)(ws + WS_MODF);
  const f32x4 sbA = *(const f32x4*)(mf + 3 * D + dA), sbB = *(const f32x4*)(mf + 3 * D + dA + 16);
  const f32x4 cbA = *(const f32x4*)(mf + 4 * D + dA), cbB = *(const f32x4*)(mf + 4 * D + dA + 16);
  u32 pf = 0;
  {
    const int i = blockIdx.x * (PF3 * 144 / 256) + tid;
    if (tid < PF3 * 144 / 256) pf = touch((const char*)(i < PF3 * 72 ? w1 : w3) + (size_t)(PF2 * 72 + (i < PF3 * 72 ? i : i - PF3 * 72)) * 128);
  }
  u32 p[4];
  p[0] = pk_bf(bflo(xa[0]) + bf2f(rv[0]), bfhi(xa[0]) + bf2f(rv[1]));
  p[1] = pk_bf(bflo(xa[1]) + bf2f(rv[2]), bfhi(xa[1]) + bf2f(rv[3]));
  p[2] = pk_bf(bflo(xb[0]) + bf2f(rv[4]), bfhi(xb[0]) + bf2f(rv[5]));
  p[3] = pk_bf(bflo(xb[1]) + bf2f(rv[6]), bfhi(xb[1]) + bf2f(rv[7]));
  float e[8];
#pragma unroll
  for (int j = 0; j < 4; ++j) { e[2 * j] = bflo(p[j]); e[2 * j + 1] = bfhi(p[j]); }
  float s = ((e[0] + e[1]) + (e[2] + e[3])) + ((e[4] + e[5]) + (e[6] + e[7]));
  s = sum_hi3(s);
  if (lane < 8) red[0][wid][lane] = s;
  __syncthreads();
  u16* x1 = (u16*)(ws + WS_X1) + (size_t)t * D + dA;
  *(u32x2*)x1 = u32x2{p[0], p[1]};
  *(u32x2*)(x1 + 16) = u32x2{p[2], p[3]};
  float mean = 0;
#pragma unroll
  for (int w = 0; w < 9; ++w) mean += red[0][w][tok];
  mean *= (1.0f / D);
  float q = 0;
#pragma unroll
  for (int j = 0; j < 8; ++j) { const float dl = e[j] - mean; q += dl * dl; }
  q = sum_hi3(q);
  if (lane < 8) red[1][wid][lane] = q;
  __syncthreads();
  float var = 0;
#pragma unroll
  for (int w = 0; w < 9; ++w) var += red[1][w][tok];
  var *= (1.0f / D);
  const float m = bfr(mean);
  const float rs = __builtin_amdgcn_rcpf(bfr(__builtin_amdgcn_sqrtf(bfr(bfr(var) + 1e-5f))));
  u32x4 o;
#pragma unroll
  for (int j = 0; j < 4; ++j) {
    const float c0 = j < 2 ? cbA[2 * j] : cbB[2 * j - 4], c1 = j < 2 ? cbA[2 * j + 1] : cbB[2 * j - 3];
    const float s0 = j < 2 ? sbA[2 * j] : sbB[2 * j - 4], s1 = j < 2 ? sbA[2 * j + 1] : sbB[2 * j - 3];
    const u32 dd = pk_bf(e[2 * j] - m, e[2 * j + 1] - m);
    const u32 xh = pk_bf(bflo(dd) * rs, bfhi(dd) * rs);
    const u32 mm = pk_bf(bflo(xh) * c0, bfhi(xh) * c1);
    o[j] = pk_bf(bflo(mm) + s0, bfhi(mm) + s1);
  }
  ((u32x4*)(ws + WS_H2T))[((size_t)(tq >> 1) * 18 + ks) * 64 + 16 * g + 8 * (tq & 1) + tok] = o;
  TOUCH_DONE(pf);
  STAMP_END(2);
}

__device__ __forceinline__ void glds16(const void* base, u32 voff, u32 lds) {
  asm volatile("s_mov_b32 m0, %2\n\ts_nop 0\n\tglobal_load_lds_dwordx4 %0, %1" ::"v"(voff), "s"(base), "s"(lds) : "memory");
}
__device__ __forceinline__ void glds16_q(const void* base, u32 voff, u32 lds) {
  asm volatile("s_mov_b32 m0, %2\n\ts_mov_b64 exec, 0xffff\n\tglobal_load_lds_dwordx4 %0, %1\n\ts_mov_b64 exec, -1" ::"v"(voff), "s"(base), "s"(lds) : "memory");
}
__device__ __forceinline__ void poll_ones(const u32* p, int wid, u32& a, u32& b) {
  u32 cnt = 1u << 19;
  asm volatile("s_cmp_lg_u32 %4, 1\n\ts_cbranch_scc1 2f\n\tglobal_load_dword %0, %3, off sc1\n"
               "1:\n\ts_sleep 4\n\tglobal_load_dword %1, %3, off sc1\n\ts_waitcnt vmcnt(1)\n\tv_cmp_ne_u32 vcc, 1, %0\n\ts_cbranch_vccz 2f\n\t"
               "s_sleep 4\n\tglobal_load_dword %0, %3, off sc1\n\ts_waitcnt vmcnt(1)\n\tv_cmp_ne_u32 vcc, 1, %1\n\ts_cbranch_vccz 2f\n\t"
               "s_sub_u32 %2, %2, 1\n\ts_cbranch_scc0 1b\n2:"
               : "=&v"(a), "=&v"(b), "+s"(cnt) : "v"(p), "s"(wid) : "vcc", "scc", "memory");
}
#define POLL_END(a, b) asm volatile("" ::"v"(a), "v"(b))
__device__ __forceinline__ int fresh_lane() {
  int l;
  asm volatile("v_mbcnt_lo_u32_b32 %0, -1, 0\n\tv_mbcnt_hi_u32_b32 %0, -1, %0" : "=v"(l));
  return l;
}
__device__ __forceinline__ u32 lds_addr(const void* p) { return (u32)(size_t)(const LAS char*)p; }
__device__ __forceinline__ bf16x8 tr_frag_a(u32 addr, int o2) {
  const s16x4 a = __builtin_amdgcn_ds_read_tr16_b64_v4i16((LAS s16x4*)(size_t)addr);
  const s16x4 b = __builtin_amdgcn_ds_read_tr16_b64_v4i16((LAS s16x4*)(size_t)(addr + o2));
  return __builtin_bit_cast(bf16x8, __builtin_shufflevector(a, b, 0, 1, 2, 3, 4, 5, 6, 7));
}
__device__ __forceinline__ bf16x8 tr_frag(const char* p, int o2) {
  const s16x4 a = __builtin_amdgcn_ds_read_tr16_b64_v4i16((LAS s16x4*)p);
  const s16x4 b = __builtin_amdgcn_ds_read_tr16_b64_v4i16((LAS s16x4*)(p + o2));
  return __builtin_bit_cast(bf16x8, __builtin_shufflevector(a, b, 0, 1, 2, 3, 4, 5, 6, 7));
}

#define GLU 1
#define CODE_P 7
#define FREE 3
#define GPRE 0
#define OPRE 0
#define K4MAP 0
#define GST 2
__device__ __forceinline__ void st16(char* p, u32x4 v) {
  asm volatile("global_store_dwordx4 %0, %1, off sc1\n\ts_nop 1" ::"v"(p), "v"(v) : "memory");
}
constexpr int G2_STAGE = 64 * 288, G2_LDS = 8 * G2_STAGE;

__device__ __forceinline__ void glds16_h(const void* base, u32 voff, u32 lds) {
  asm volatile("s_mov_b32 m0, %2\n\ts_mov_b32 exec_hi, 0\n\tglobal_load_lds_dwordx4 %0, %1\n\ts_mov_b32 exec_hi, -1" ::"v"(voff), "s"(base), "s"(lds) : "memory");
}
__device__ __forceinline__ constexpr int slab_row(int sg) { return 288 * (sg / 9) + (sg % 9 < 8 ? 72 * ((sg % 9) >> 1) + 32 * ((sg % 9) & 1) : 64); }

constexpr int G1_W1 = 16384, G1_W3 = G1_W1 + 9216 + 16, G1_STAGE = 35072, G1_LDS = 4 * G1_STAGE;

extern "C" __global__ __launch_bounds__(512, 2) void k4_kernel(const u16* __restrict__ w1, const u16* __restrict__ w3, const u16* __restrict__ w2,
                                                               char* __restrict__ ws, u16* __restrict__ out) {
  extern __shared__ __attribute__((aligned(16))) char smem[];
  const int tid = threadIdx.x, lane = tid & 63, wid = __builtin_amdgcn_readfirstlane(tid >> 6);
  STAMP_BEGIN();
  const int mt = (blockIdx.x >> 3) & 7, ft = (blockIdx.x & 7) * 4 + (blockIdx.x >> 6);
  const int wm = wid & 3, wn = wid >> 2;
  const char* tsrc = ws + WS_H2T + (size_t)(16 * mt + 2 * wid) * 18 * 1024;
  const char* wsrc = (const char*)(wn ? w3 : w1) + (size_t)(8 * wm) * (F * 2) + ft * 288;
  const int x1 = 64 + lane, x2 = 128 + (lane & 15);
  const u32 o0 = (lane / 18) * (F * 2) + (lane % 18) * 16, o1 = (x1 / 18) * (F * 2) + (x1 % 18) * 16, o2 = (x2 / 18) * (F * 2) + (x2 % 18) * 16;
  const u32 ol = lane * 16;
  const u32 tdst = lds_addr(smem) + 2 * wid * 1024, wdst = lds_addr(smem) + (wn ? G1_W3 : G1_W1) + wm * 2304;
  auto stage = [&](int p) {
    const u32 sb = (p & 3) * G1_STAGE;
    glds16(tsrc + p * 1024, ol, sb + tdst);
    glds16(tsrc + (18 + p) * 1024, ol, sb + tdst + 1024);
    const char* wp = wsrc + (size_t)p * 32 * (F * 2);
    glds16(wp, o0, sb + wdst);
    glds16(wp, o1, sb + wdst + 1024);
    glds16_q(wp, o2, sb + wdst + 2048);
  };
  const int g = lane >> 4, q = (lane >> 2) & 3, pc = lane & 3;
  u32 boff = lds_addr(smem) + wm * 4096 + lane * 16;
  u32 aoff = lds_addr(smem) + G1_W1 + (4 * g + q) * 288 + wn * 144 + 8 * pc;
  u32 moff = lds_addr(smem) + (pc < 2 ? G1_W1 : G1_W3) + (4 * g + q) * 288 + wn * 144 + 128 + 8 * (pc & 1);
  f32x4 acc[9][4];
#pragma unroll
  for (int i = 0; i < 9; ++i)
#pragma unroll
    for (int j = 0; j < 4; ++j) acc[i][j] = f32x4{0, 0, 0, 0};

  stage(0); stage(1); stage(2);
  const char* wbase;
  u32 wvo;
  {
    constexpr u32 NL = (D - PF_ROWS) * 3 / 8;
    unsigned long long pc0;
    u32 len;
    asm volatile("s_getpc_b64 %0\n.Lk4_here:\n\ts_mov_b32 %1, kz_end-.Lk4_here" : "=s"(pc0), "=s"(len));
    const u32 i = mt * NL + min((wid & 1) * 64u + lane, NL - 1);
    wbase = wid < 4 ? (const char*)(wid < 2 ? w1 : w3) + (size_t)PF_ROWS * (F * 2) + ((ft * 288) & ~127)
                    : wid < 7 ? (const char*)w2 + (size_t)blockIdx.x * 162 * 128 : (const char*)pc0;
    wvo = wid < 4 ? (i / 3) * (F * 2) + (i % 3) * 128
                  : wid < 7 ? min((wid - 4) * 64u + lane, 161u) * 128 : min((((blockIdx.x >> 3) & 7) * 64 + lane) * 128u, len - 4);
  }
  const u32 wlds = lds_addr(smem) + G1_LDS + wid * 256;
  WAIT_V(10); BAR();
  STAMP_MID(0);
  if (wn) BAR();
#pragma unroll
  for (int p = 0; p < 18; ++p) {
    asm volatile("" : "+v"(boff), "+v"(aoff), "+v"(moff));
    bf16x8 A[9], B[4];
#pragma unroll
    for (int j = 0; j < 4; ++j) B[j] = *(const LAS bf16x8*)(size_t)(boff + j * 1024);
#pragma unroll
    for (int i = 0; i < 4; ++i) {
      A[i] = tr_frag_a(aoff + 32 * i, 16 * 288);
      A[4 + i] = tr_frag_a(aoff + (G1_W3 - G1_W1) + 32 * i, 16 * 288);
    }
    A[8] = tr_frag_a(moff, 16 * 288);
    {
      const u32 adv = (p & 3) == 3 ? -3 * G1_STAGE : G1_STAGE;
      boff += adv; aoff += adv; moff += adv;
    }
    if (p + 3 < 18) stage(p + 3);
    if (p < 8)
      asm volatile("s_mov_b32 m0, %2\n\ts_cmp_eq_u32 %3, %4\n\ts_cselect_b64 exec, -1, 0\n\tglobal_load_lds_dword %0, %1\n\ts_mov_b64 exec, -1"
                   ::"v"(wvo), "s"(wbase), "s"(wlds), "s"(wid), "i"(p) : "memory", "scc");
    if (OPRE && p == 12) {
      const u32 l = fresh_lane();
      const int kq_ = blockIdx.x & 3, m2_ = ((blockIdx.x >> 2) & 1) * 8 + ((blockIdx.x >> 3) & 7);
      asm volatile("s_mov_b32 m0, %2\n\ts_nop 0\n\tglobal_load_lds_dword %0, %1" ::"v"((l & 15) * (D * 2) + min(l >> 4, 2u) * 128),
                   "s"((const char*)out + ((size_t)(128 * m2_ + 32 * kq_ + 16 * (wid & 1)) * D * 2 + (((blockIdx.x >> 6) * 288) & ~127))), "s"(wlds) : "memory");
    }
    if (GPRE && p >= GPRE && p < GPRE + 8) {
      const char* gp = ws + WS_GT + ((size_t)(16 * mt + 4 * wm + ((p - GPRE) >> 1)) * 144 + 9 * (ft >> 1) + 2 * (2 * (ft & 1) + wn) + ((p - GPRE) & 1)) * 1024;
      asm volatile("global_store_dwordx4 %0, %1, %2 sc1\n\ts_nop 1" ::"v"(ol), "v"(acc[0][0]), "s"(gp) : "memory");
    }
    constexpr int NP = 18 - FREE;
    if (p < NP) {
      const int NC = GPRE ? (p - 2 >= GPRE && p - 2 < GPRE + 8) + (p - 1 >= GPRE && p - 1 < GPRE + 8) + (p >= GPRE && p < GPRE + 8) : 0;
      if (p < 15) asm volatile("s_waitcnt vmcnt(%0)" ::"i"(10 + NC) : "memory"); else if (p == 15) WAIT_V(5); else if (p == 16) WAIT_V(0);
      WAIT_L0(); SCHED();
      BAR();
      __builtin_amdgcn_s_setprio(1);
    }
#pragma unroll
    for (int i = 0; i < 9; ++i)
#pragma unroll
      for (int j = 0; j < 4; ++j)
        asm volatile("v_mfma_f32_16x16x32_bf16 %0, %1, %2, %0" : "+v"(acc[i][j]) : "v"(A[i]), "v"(B[j]));
    if (p < NP) __builtin_amdgcn_s_setprio(0);
    SCHED();
    if (p < NP - 1) BAR();
    else if (p == NP - 1) {
      if (wn) WAIT_V(0);
      if (FREE || !wn) BAR();
      if (FREE && !wn) { WAIT_V(0); BAR(); __builtin_amdgcn_s_setprio(2); }
    }
  }
  asm volatile("s_nop 7\n\ts_nop 7" ::: "memory");
  SCHED();
  const int ln = fresh_lane(), g2 = ln >> 4, q2 = (ln >> 2) & 3, pc2 = ln & 3;
  const u32 ol2 = ln * 16;
  const int kq = blockIdx.x & 3, m2 = ((blockIdx.x >> 2) & 1) * 8 + ((blockIdx.x >> 3) & 7), cb = blockIdx.x >> 6;
  const int kg = wn, hs = wm >> 1;
  const char* gsrc = ws + WS_GT + ((size_t)(8 * m2 + 2 * wm) * 144 + 36 * kq + 18 * kg) * 1024;
  const char* w2src = (const char*)w2 + (size_t)(1152 * kq + 576 * kg) * (D * 2) + cb * 288;
  u32 wo[5];
#pragma unroll
  for (int i = 0; i < 5; ++i) { const int x = 64 * i + (i < 4 ? ln : ln & 31); wo[i] = (x / 18) * (D * 2) + (x % 18) * 16; }
  const u32 wo2l = wo[2] + (ln >= 16 ? 64 * (D * 2) : 0);
  const u32 w2dst = lds_addr(smem) + kg * 4 * G2_STAGE + wm * 4608;
  u32x4 B[4][4];
  auto issue = [&](int j, int i) {
    if (i < 5) {
      const bool left = hs ? (2 * j + 1) % 9 == 8 : (2 * j) % 9 == 8;
      const int row = (hs ? slab_row(2 * j + 1) : slab_row(2 * j)) + (left ? 144 : 16) * (wm & 1) + (left && i > 2 ? 64 : 0);
      const char* p = w2src + (size_t)row * (D * 2);
      const u32 dst = w2dst + (j & 3) * G2_STAGE + i * 1024;
      if (i == 4) glds16_h(p, wo[4], dst);
      else if (i == 2 && (j == 4 || j == 8)) glds16(p, left ? wo2l : wo[2], dst);
      else glds16(p, wo[i], dst);
    } else {
      const int f = i - 5;
      asm volatile("global_load_dwordx4 %0, %1, %2 sc1" : "=v"(B[j & 3][f]) : "v"(ol2), "s"(gsrc + ((size_t)(f & 1) * 144 + 2 * j + (f >> 1)) * 1024) : "memory");
    }
  };
  STAMP_MID(1);
  const int P = ft >> 1, qq = 2 * (ft & 1) + wn;
  char* gbase = ws + WS_GT + ((size_t)(16 * mt + 4 * wm) * 144 + 9 * P) * 1024;
  auto glu2 = [&](float g0, float g1, float u0, float u1) -> u32 {
    const f32x2 gv = {g0, g1}, uv = {u0, u1};
    const f32x2 t = gv * -1.4426950408889634f;
    const f32x2 dn = f32x2{__builtin_amdgcn_exp2f(t[0]), __builtin_amdgcn_exp2f(t[1])} + 1.0f;
    const f32x2 o = (gv * uv) * f32x2{__builtin_amdgcn_rcpf(dn[0]), __builtin_amdgcn_rcpf(dn[1])};
    return pk_bf(o[0], o[1]);
  };
#pragma unroll
  for (int j = 0; j < 4; ++j)
#pragma unroll
    for (int s = 0; s < 2; ++s) {
      u32x4 o;
#pragma unroll
      for (int h = 0; h < 2; ++h) {
        const f32x4 a = acc[2 * s + h][j], u = acc[4 + 2 * s + h][j];
        o[2 * h] = glu2(a[0], a[1], u[0], u[1]);
        o[2 * h + 1] = glu2(a[2], a[3], u[2], u[3]);
      }
      st16(gbase + (size_t)j * 144 * 1024 + (2 * qq + s) * 1024 + ln * 16, o);
    }
#pragma unroll
  for (int jp = 0; jp < 2; ++jp) {
    float a[4], u[4];
#pragma unroll
    for (int e = 0; e < 4; ++e) {
      const float m0 = acc[8][2 * jp][e], m1 = acc[8][2 * jp + 1][e];
      auto r = __builtin_amdgcn_permlane32_swap(__builtin_bit_cast(u32, m0), __builtin_bit_cast(u32, m1), false, false);
      a[e] = __builtin_bit_cast(float, (u32)r[0]); u[e] = __builtin_bit_cast(float, (u32)r[1]);
    }
    const u32x2 o = {glu2(a[0], a[1], u[0], u[1]), glu2(a[2], a[3], u[2], u[3])};
    const int j = 2 * jp + (ln >> 5);
    char* gp = gbase + (size_t)j * 144 * 1024 + 8 * 1024 + (16 * (2 * (qq & 1) + (g2 & 1)) + (ln & 15)) * 16 + 8 * (qq >> 1);
    asm volatile("global_store_dwordx2 %0, %1, off sc1\n\ts_nop 1" ::"v"(gp), "v"(o) : "memory");
  }
  u32 pa, pb;
  __builtin_amdgcn_s_setprio(0);
  WAIT_V(0); BAR();
  STAMP_MID(2);
  {
    u32* gfl = (u32*)(ws + WS_FLAG) + 8192;
    if (wid == 0) {
      if (ln == 0) __hip_atomic_store(gfl + (mt * 4 + (ft >> 3)) * 32 + (ft & 7), 1u, __ATOMIC_RELAXED, __HIP_MEMORY_SCOPE_AGENT);
    }
#pragma unroll
    for (int j = 0; j < 2; ++j)
#pragma unroll
      for (int i = 0; i < 5; ++i) issue(j, i);
    poll_ones(gfl + ((m2 >> 1) * 4 + kq) * 32 + (ln & 7), wid, pa, pb);
    BAR();
  }
#define WAIT_B(n, j) asm volatile("s_waitcnt vmcnt(" #n ")" : "+v"(B[(j) & 3][0]), "+v"(B[(j) & 3][1]), "+v"(B[(j) & 3][2]), "+v"(B[(j) & 3][3])::"memory")
  const char* abase = smem + kg * 4 * G2_STAGE + (4 * g2 + q2) * 288 + 8 * pc2;
  f32x4 ac2[9][2];
#pragma unroll
  for (int c = 0; c < 9; ++c) ac2[c][0] = ac2[c][1] = f32x4{0, 0, 0, 0};
#pragma unroll
  for (int i = 5; i < 9; ++i) issue(0, i);
  WAIT_V(9); BAR();
  STAMP_MID(3);
  if (kg) BAR();
#pragma unroll
  for (int j = 0; j < 9; ++j) {
    bf16x8 A[2][9];
#pragma unroll
    for (int h = 0; h < 2; ++h)
#pragma unroll
      for (int c = 0; c < 9; ++c) A[h][c] = tr_frag(abase + (j & 3) * G2_STAGE + h * 32 * 288 + 32 * c, 16 * 288);
#pragma unroll
    for (int i = 5; i < 9; ++i)
      if (j < 8) issue(j + 1, i);
    if (j == 0) WAIT_B(4, j); else if (j < 8) WAIT_B(9, j); else WAIT_B(0, j);
    if (j == 0) POLL_END(pa, pb);
    WAIT_L0(); SCHED();
    BAR();
    __builtin_amdgcn_s_setprio(1);
#pragma unroll
    for (int i = 0; i < 9; ++i) {
#pragma unroll
      for (int e = 0; e < 2; ++e) {
        const int h = (2 * i + e) / 9, c = (2 * i + e) % 9;
#pragma unroll
        for (int t = 0; t < 2; ++t) {
          const u32x4 bv = B[j & 3][2 * h + t];
          ac2[c][t] = __builtin_amdgcn_mfma_f32_16x16x32_bf16(A[h][c], __builtin_bit_cast(bf16x8, bv), ac2[c][t], 0, 0, 0);
        }
      }
      SCHED();
      if (j + 2 < 9 && !(i & 1)) issue(j + 2, i >> 1);
      SCHED();
    }
    __builtin_amdgcn_s_setprio(0);
    if (j < 7) WAIT_V(9); else if (j == 7) WAIT_V(4);
    SCHED();
    if (j < 8 || !kg) BAR();
  }
  STAMP_MID(4);
  const int et = kg, ep = wm;
  const size_t orow = (size_t)(128 * m2 + 32 * kq + 16 * et + (ln & 15)) * D + 144 * cb + 4 * g2;
  f32x4 gb[3]; u32x2 xr[3];
#pragma unroll
  for (int c = 0; c < 3; ++c) {
    const int ct = c < 2 ? 2 * ep + c : 8;
    gb[c] = *(const f32x4*)((const float*)(ws + WS_MODF) + 5 * D + 144 * cb + 16 * ct + 4 * g2);
    xr[c] = *(const u32x2*)((const u16*)(ws + WS_X1) + orow + 16 * ct);
  }
  f32x4* red = (f32x4*)smem;
#pragma unroll
  for (int c = 0; c < 9; ++c) red[(wid * 9 + c) * 64 + ln] = kg ? ac2[c][0] : ac2[c][1];
  WAIT_L0(); BAR();
  const int tile = m2 * 4 + cb;
  char* xch = ws + WS_XCH + (size_t)tile * (12 * 9216);
  f32x4* own = red + 72 * 64;
  {
    f32x4 v[9];
#pragma unroll
    for (int c = 0; c < 9; ++c) v[c] = (kg ? ac2[c][1] : ac2[c][0]) + red[((wid ^ 4) * 9 + c) * 64 + ln];
    if (wm == kq) {
#pragma unroll
      for (int c = 0; c < 9; ++c) own[(kg * 9 + c) * 64 + ln] = v[c];
    } else {
      char* p = xch + ((wm * 3 + (kq > wm ? kq - 1 : kq)) * 2 + kg) * 4608;
#pragma unroll
      for (int c = 0; c < 4; ++c) {
        const u32x4 w = {pk_bf(v[2 * c][0], v[2 * c][1]), pk_bf(v[2 * c][2], v[2 * c][3]), pk_bf(v[2 * c + 1][0], v[2 * c + 1][1]),
                         pk_bf(v[2 * c + 1][2], v[2 * c + 1][3])};
        asm volatile("global_store_dwordx4 %0, %1, off sc1\n\ts_nop 1" ::"v"(p + c * 1024 + ln * 16), "v"(w) : "memory");
      }
      const u32x2 w = {pk_bf(v[8][0], v[8][1]), pk_bf(v[8][2], v[8][3])};
      asm volatile("global_store_dwordx2 %0, %1, off sc1\n\ts_nop 1" ::"v"(p + 4096 + ln * 8), "v"(w) : "memory");
    }
  }
  asm volatile("s_waitcnt vmcnt(0) lgkmcnt(0)" : "+v"(gb[0]), "+v"(gb[1]), "+v"(gb[2]), "+v"(xr[0]), "+v"(xr[1]), "+v"(xr[2])::"memory");
  BAR();
  u32* flags = (u32*)(ws + WS_FLAG) + tile * 128;
  if (wid == 0) {
    if (ln == 0) {
#pragma unroll
      for (int r = 0; r < 4; ++r)
        if (r != kq) __hip_atomic_store(flags + r * 32 + (kq > r ? kq - 1 : kq), 1u, __ATOMIC_RELAXED, __HIP_MEMORY_SCOPE_AGENT);
    }
  }
  poll_ones(flags + kq * 32 + min(ln, 2), wid, pa, pb);
  BAR();
  STAMP_MID(5);
  {
    u32x4 pr[3]; u32x2 pl[3];
    const char* src = xch + ((size_t)kq * 6 + et) * 4608;
    const char *s4 = src + ep * 1024 + ln * 16, *s2 = src + 4096 + ln * 8;
    asm volatile("global_load_dwordx4 %0, %6, off sc1\n\tglobal_load_dwordx4 %1, %7, off sc1\n\tglobal_load_dwordx4 %2, %8, off sc1\n\t"
                 "global_load_dwordx2 %3, %9, off sc1\n\tglobal_load_dwordx2 %4, %10, off sc1\n\tglobal_load_dwordx2 %5, %11, off sc1\n\t"
                 "s_waitcnt vmcnt(0)"
                 : "=&v"(pr[0]), "=&v"(pr[1]), "=&v"(pr[2]), "=&v"(pl[0]), "=&v"(pl[1]), "=&v"(pl[2])
                 : "v"(s4), "v"(s4 + 9216), "v"(s4 + 18432), "v"(s2), "v"(s2 + 9216), "v"(s2 + 18432) : "memory");
    POLL_END(pa, pb);
#pragma unroll
    for (int c = 0; c < 3; ++c) {
      const int ct = c < 2 ? 2 * ep + c : 8;
      f32x4 s = own[(et * 9 + ct) * 64 + ln];
#pragma unroll
      for (int k = 0; k < 3; ++k) {
        const u32 lo = c < 2 ? pr[k][2 * c] : pl[k][0], hi = c < 2 ? pr[k][2 * c + 1] : pl[k][1];
        s += f32x4{bflo(lo), bfhi(lo), bflo(hi), bfhi(hi)};
      }
      u32x2 o;
#pragma unroll
      for (int h = 0; h < 2; ++h) {
        const u32 y = pk_bf(s[2 * h], s[2 * h + 1]);
        const u32 z = pk_bf(gb[c][2 * h] * bflo(y), gb[c][2 * h + 1] * bfhi(y));
        o[h] = pk_bf(bflo(xr[c][h]) + bflo(z), bfhi(xr[c][h]) + bfhi(z));
      }
      if (c < 2 || ep == 0) *(u32x2*)(out + orow + 16 * ct) = o;
    }
  }
  STAMP_END(3);
}

extern "C" __device__ __attribute__((used, noinline)) void kz_end() {}

extern "C" void kernel_launch(void* const* d_in, const int* in_sizes, int n_in, void* d_out, int out_size, void* d_ws, size_t ws_size,
                              hipStream_t stream) {
  const u16* u = (const u16*)d_in[0];
  char* ws = (char*)d_ws;
  (void)hipFuncSetAttribute((const void*)k4_kernel, hipFuncAttributeMaxDynamicSharedMemorySize, G2_LDS);
  k1_kernel<<<K1_MOD + K1_LN + K1_KG, 256, 0, stream>>>(u, (const u16*)d_in[1], (const u16*)d_in[2], (const u16*)d_in[3], (const u16*)d_in[4],
                                                        (const u16*)d_in[5], (const u16*)d_in[6], (const u16*)d_in[7], (const u16*)d_in[13],
                                                        (const u16*)d_in[14], ws);
  k2_kernel<<<D, 256, 0, stream>>>((const u16*)d_in[8], (const u16*)d_in[10], (const u16*)d_in[11], ws);
  k3_kernel<<<256, 576, 0, stream>>>(u, (const u16*)d_in[10], (const u16*)d_in[11], ws);
  k4_kernel<<<256, 512, G2_LDS, stream>>>((const u16*)d_in[10], (const u16*)d_in[11], (const u16*)d_in[12], ws, (u16*)d_out);
}
```

```cpp
#include <hip/hip_runtime.h>
#include <hip/hip_bf16.h>

typedef unsigned short u16;
typedef unsigned int u32;
typedef __attribute__((ext_vector_type(2))) float f32x2;
typedef __attribute__((ext_vector_type(4))) float f32x4;
typedef __attribute__((ext_vector_type(2))) u32 u32x2;
typedef __attribute__((ext_vector_type(4))) u32 u32x4;
typedef __attribute__((ext_vector_type(4))) short s16x4;
typedef __attribute__((ext_vector_type(8))) short s16x8;
typedef __attribute__((ext_vector_type(2))) __bf16 bf16x2;
typedef __attribute__((ext_vector_type(8))) __bf16 bf16x8;
typedef __attribute__((ext_vector_type(2))) _Float16 f16x2;
typedef __attribute__((ext_vector_type(8))) _Float16 f16x8;
#define LAS __attribute__((address_space(3)))
#define GAS __attribute__((address_space(1)))

constexpr int L = 2048, D = 576, NM = 128, F = 4608, MODN = 6 * D;
constexpr int KSPLIT = 3;

constexpr size_t WS_MODP = 0;
constexpr size_t WS_MODF = 65536;
constexpr size_t WS_XHT  = 131072;
constexpr size_t ACT_B   = (size_t)L * D * 2;
constexpr size_t WS_KX   = WS_XHT + ACT_B;
constexpr size_t WS_RT   = WS_KX + ACT_B;
constexpr size_t WS_X1   = WS_RT + ACT_B;
constexpr size_t WS_H2T  = WS_X1 + ACT_B;
constexpr size_t WS_GT   = WS_H2T + ACT_B;
constexpr size_t WS_XCH  = WS_GT + (size_t)L * F * 2;
constexpr size_t WS_FLAG = 81920;

#define WAIT_V(n) asm volatile("s_waitcnt vmcnt(" #n ")" ::: "memory")
#define WAIT_L0() asm volatile("s_waitcnt lgkmcnt(0)" ::: "memory")
#define BAR() __builtin_amdgcn_s_barrier()
#define SCHED() __builtin_amdgcn_sched_barrier(0)
__device__ __forceinline__ float bf2f(u16 v) { return __builtin_bit_cast(float, (u32)v << 16); }
__device__ __forceinline__ float bflo(u32 v) { return __builtin_bit_cast(float, v << 16); }
__device__ __forceinline__ float bfhi(u32 v) { return __builtin_bit_cast(float, v & 0xffff0000u); }
__device__ __forceinline__ u32 pk_bf(float a, float b) { f32x2 v = {a, b}; return __builtin_bit_cast(u32, __builtin_convertvector(v, bf16x2)); }
__device__ __forceinline__ u32 pk_h(float a, float b) { f32x2 v = {a, b}; return __builtin_bit_cast(u32, __builtin_convertvector(v, f16x2)); }
__device__ __forceinline__ float bfr(float a) { return bflo(pk_bf(a, a)); }

template <int CTRL> __device__ __forceinline__ float dpp(float v) {
  return __builtin_bit_cast(float, __builtin_amdgcn_update_dpp(0, __builtin_bit_cast(int, v), CTRL, 0xf, 0xf, true));
}
__device__ __forceinline__ float sum16x(float v) {
  u32 u = __builtin_bit_cast(u32, v);
  auto r = __builtin_amdgcn_permlane16_swap(u, u, false, false);
  return __builtin_bit_cast(float, (u32)r[0]) + __builtin_bit_cast(float, (u32)r[1]);
}
__device__ __forceinline__ float sum32x(float v) {
  u32 u = __builtin_bit_cast(u32, v);
  auto r = __builtin_amdgcn_permlane32_swap(u, u, false, false);
  return __builtin_bit_cast(float, (u32)r[0]) + __builtin_bit_cast(float, (u32)r[1]);
}
__device__ __forceinline__ float wave_sum(float v) {
  v += dpp<0xB1>(v);
  v += dpp<0x4E>(v);
  v += dpp<0x141>(v);
  v += dpp<0x140>(v);
  return sum32x(sum16x(v));
}
__device__ __forceinline__ float sum_hi3(float v) {
  v += dpp<0x128>(v);
  return sum32x(sum16x(v));
}
__device__ __forceinline__ float silu_f(float g) { return g * __builtin_amdgcn_rcpf(1.0f + __builtin_amdgcn_exp2f(-1.4426950408889634f * g)); }

__device__ __forceinline__ u32 touch(const void* p) {
  u32 r;
  asm volatile("global_load_dword %0, %1, off" : "=v"(r) : "v"(p) : "memory");
  return r;
}
#define TOUCH_DONE(r) asm volatile("s_waitcnt vmcnt(0)" : "+v"(r)::"memory")

constexpr int K1_MOD = 54 * KSPLIT, K1_LN = 256, K1_KG = D;
constexpr int TROW = 528;

typedef f32x2 cplx;
__device__ __forceinline__ cplx cmul(cplx a, cplx z) {
  cplx t, r;
  asm("v_pk_mul_f32 %0, %1, %2 op_sel_hi:[1,0]" : "=v"(t) : "v"(a), "v"(z));
  asm("v_pk_fma_f32 %0, %1, %2, %3 op_sel:[1,1,0] op_sel_hi:[0,1,1] neg_lo:[1,0,0]" : "=v"(r) : "v"(a), "v"(z), "v"(t));
  return r;
}
__device__ __forceinline__ cplx cexpl(float al2, float brev, float l) {
  const float m = __builtin_amdgcn_exp2f(al2 * l);
  const float ph = __builtin_amdgcn_fractf(brev * l);
  return cplx{m * __builtin_amdgcn_cosf(ph), m * __builtin_amdgcn_sinf(ph)};
}

extern "C" __global__ __launch_bounds__(256) void k1_kernel(const u16* __restrict__ x, const u16* __restrict__ Are, const u16* __restrict__ Aim,
                                                 const u16* __restrict__ Bre, const u16* __restrict__ Bim, const u16* __restrict__ Cre,
                                                 const u16* __restrict__ Cim, const u16* __restrict__ logdt, const u16* __restrict__ cvec,
                                                 const u16* __restrict__ wmod, char* __restrict__ ws) {
  __shared__ __attribute__((aligned(16))) char smem[64 * TROW + 2624];
  const int tid = threadIdx.x, lane = tid & 63, wid = tid >> 6;
  int b = blockIdx.x;
  u32 pf = 0;
  if (b < 16) {
    unsigned long long pc;
    u32 len;
    asm volatile("s_getpc_b64 %0\n.Lk1_here:\n\ts_mov_b32 %1, kz_end-.Lk1_here" : "=s"(pc), "=s"(len));
    const u32 o = ((b >> 3) * 256 + tid) * 128;
    if (o < len) pf = touch((const void*)(pc + o));
  }
  b = b < K1_KG ? b + K1_MOD + K1_LN : b - K1_KG;
  if (b < K1_MOD) {
    const int cb = b % 54, kq = b / 54, cc = tid & 7, rg = tid >> 3;
    u32x4 w[6]; u16 cv[6];
#pragma unroll
    for (int i = 0; i < 6; ++i) {
      const int row = 192 * kq + rg + 32 * i;
      w[i] = *(const u32x4*)(wmod + (size_t)row * MODN + 64 * cb + 8 * cc);
      cv[i] = cvec[row];
    }
    float acc[8] = {0, 0, 0, 0, 0, 0, 0, 0};
#pragma unroll
    for (int i = 0; i < 6; ++i) {
      const float s = bfr(silu_f(bf2f(cv[i])));
#pragma unroll
      for (int j = 0; j < 4; ++j) { acc[2 * j] += s * bflo(w[i][j]); acc[2 * j + 1] += s * bfhi(w[i][j]); }
    }
#pragma unroll
    for (int j = 0; j < 8; ++j) acc[j] = sum_hi3(acc[j]);
    float* red = (float*)smem;
    if (lane < 8) {
      *(f32x4*)(red + wid * 64 + lane * 8) = f32x4{acc[0], acc[1], acc[2], acc[3]};
      *(f32x4*)(red + wid * 64 + lane * 8 + 4) = f32x4{acc[4], acc[5], acc[6], acc[7]};
    }
    __syncthreads();
    if (tid < 64) ((float*)(ws + WS_MODP))[((cb % 9) * 64 + tid) * 24 + (cb / 9) * 4 + kq] = (red[tid] + red[64 + tid]) + (red[128 + tid] + red[192 + tid]);
    return;
  }
  b -= K1_MOD;
  if (b < K1_LN) {
    u32 v[2][4]; u16 t[2];
#pragma unroll
    for (int r = 0; r < 2; ++r) {
      const u16* row = x + (size_t)(8 * b + 2 * wid + r) * D;
#pragma unroll
      for (int i = 0; i < 4; ++i) v[r][i] = *(const u32*)(row + 2 * (lane + 64 * i));
      t[r] = row[512 + lane];
    }
    u32 o[2][5];
#pragma unroll
    for (int r = 0; r < 2; ++r) {
      float e[9];
#pragma unroll
      for (int i = 0; i < 4; ++i) { e[2 * i] = bflo(v[r][i]); e[2 * i + 1] = bfhi(v[r][i]); }
      e[8] = bf2f(t[r]);
      float s = 0;
#pragma unroll
      for (int i = 0; i < 9; ++i) s += e[i];
      const float mean = wave_sum(s) * (1.0f / D);
      float q = 0;
#pragma unroll
      for (int i = 0; i < 9; ++i) { const float dlt = e[i] - mean; q += dlt * dlt; }
      const float var = wave_sum(q) * (1.0f / D);
      const float m = bfr(mean);
      const float rs = __builtin_amdgcn_rcpf(bfr(__builtin_amdgcn_sqrtf(bfr(bfr(var) + 1e-5f))));
#pragma unroll
      for (int i = 0; i < 4; ++i) {
        const u32 dd = pk_bf(e[2 * i] - m, e[2 * i + 1] - m);
        o[r][i] = pk_bf(bflo(dd) * rs, bfhi(dd) * rs);
      }
      o[r][4] = pk_bf(bfr(e[8] - m) * rs, 0.f);
    }
    u32* tile = (u32*)smem;
#pragma unroll
    for (int i = 0; i < 4; ++i) {
      tile[(2 * (lane + 64 * i)) * 4 + wid] = __builtin_amdgcn_perm(o[1][i], o[0][i], 0x05040100);
      tile[(2 * (lane + 64 * i) + 1) * 4 + wid] = __builtin_amdgcn_perm(o[1][i], o[0][i], 0x07060302);
    }
    tile[(512 + lane) * 4 + wid] = __builtin_amdgcn_perm(o[1][4], o[0][4], 0x05040100);
    __syncthreads();
    u32x4* dst = (u32x4*)(ws + WS_XHT) + (size_t)b * D;
#pragma unroll
    for (int i = 0; i < 3; ++i) {
      const int c = tid + 256 * i;
      if (c < D) dst[c] = *(const u32x4*)(tile + c * 4);
    }
    return;
  }
  b -= K1_LN;
  {
    const int n = tid & 127, half = __builtin_amdgcn_readfirstlane(tid >> 7);
    const int idx = b * NM + n;
    const float are = bf2f(Are[idx]), aim = bf2f(Aim[idx]), bre = bf2f(Bre[idx]), bim = bf2f(Bim[idx]);
    const float cre = bf2f(Cre[idx]), cim = bf2f(Cim[idx]);
    const float dt = bfr(__builtin_amdgcn_exp2f(1.4426950408889634f * bf2f(logdt[b])));
    const float al2 = (dt * are) * 1.4426950408889634f, brev = (dt * aim) * 0.15915494309189535f;
    cplx c = cmul(cplx{cre, cim}, cplx{dt * bre, dt * bim});
    const int he = (__builtin_amdgcn_frexp_expf(fmaxf(fabsf(c[0]), fabsf(c[1])))) >> 1;
    c = cplx{__builtin_amdgcn_ldexpf(c[0], -he), __builtin_amdgcn_ldexpf(c[1], -he)};
    char* Ut = smem + n * 4;
#define PUT(row, z) (*(u32*)(Ut + (row) * TROW) = pk_h((z)[0], (z)[1]))
    const cplx z128 = cexpl(al2, brev, 128.f);
    if (half == 0) {
      const cplx z8c = cexpl(al2, -brev, 8.f);
      cplx v = {__builtin_amdgcn_ldexpf(1.0f, he), 0.f};
      cplx u = cmul(c, cexpl(al2, brev, 1024.f));
#pragma unroll
      for (int j = 0; j < 16; ++j) {
        PUT(48 + j, v); if (j < 15) v = cmul(v, z8c);
        if (j < 9) { PUT(32 + j, u); if (j < 8) u = cmul(u, z128); }
      }
      const cplx bd = {fabsf(c[0]) + fabsf(c[1]), 0.f};
      PUT(41, bd);
    } else {
      const cplx z1 = cexpl(al2, brev, 1.f);
      cplx u[8];
      u[0] = c;
#pragma unroll
      for (int j = 1; j < 8; ++j) u[j] = cmul(u[j - 1], z1);
#pragma unroll
      for (int j = 0; j < 8; ++j) { PUT(j, u[j]); u[j] = cmul(u[j], z128); PUT(8 + j, u[j]); }
#pragma unroll
      for (int s = 0; s < 2; ++s)
#pragma unroll
        for (int j = 0; j < 4; ++j) { u[2 * j] = cmul(u[2 * j], z128); PUT(16 + 4 * s + j, u[2 * j]); }
#pragma unroll
      for (int s = 0; s < 4; ++s)
#pragma unroll
        for (int j = 0; j < 2; ++j) { u[4 * j] = cmul(u[4 * j], z128); PUT(24 + 2 * s + j, u[4 * j]); }
    }
#undef PUT
    WAIT_L0(); BAR();
    float* Kc = (float*)(smem + 64 * TROW);
    const int wu = __builtin_amdgcn_readfirstlane(wid);
    if (wu < 3) {
      const int r = lane & 15, g = lane >> 4;
      f32x4 acc = {0, 0, 0, 0};
#pragma unroll
      for (int ks = 0; ks < 8; ++ks) {
        const f16x8 a = *(const f16x8*)(smem + (16 * wu + r) * TROW + ks * 64 + g * 16);
        const f16x8 v = *(const f16x8*)(smem + (48 + r) * TROW + ks * 64 + g * 16);
        acc = __builtin_amdgcn_mfma_f32_16x16x32_f16(a, v, acc, 0, 0, 0);
      }
      int o0, sA = 1, sB;
      if (wu == 0) { o0 = (g < 2 ? 4 * g : 120 + 4 * g) + 8 * r; sB = 2; }
      else if (wu == 1) { o0 = g < 2 ? 256 + 64 * g + 4 * r : 256 + 64 * g + 2 * r; sB = g < 2 ? 2 : 32; }
      else { o0 = g < 2 ? 512 + 64 * g + r : (lane == 32 ? 640 : 648); sA = g < 2 ? 16 : 1; sB = g < 2 ? 32 : 4; }
      Kc[o0] = acc[0]; Kc[o0 + sA] = acc[1]; Kc[o0 + sB] = acc[2]; Kc[o0 + sA + sB] = acc[3];
    }
    WAIT_L0(); BAR();
    {
      const int q = tid;
      float k[8];
      if (q < 32) {
#pragma unroll
        for (int e = 0; e < 8; ++e) k[e] = Kc[8 * q + e];
      } else if (q < 64) {
        const float* p = Kc + 256 + 4 * (q - 32);
        k[0] = k[1] = p[0]; k[2] = p[1]; k[3] = k[4] = k[5] = p[2]; k[6] = p[3]; k[7] = p[4];
      } else if (q < 128) {
        const float* p = Kc + 384 + 2 * (q - 64);
        k[0] = k[1] = k[2] = p[0]; k[3] = k[4] = k[5] = p[1]; k[6] = k[7] = p[2];
      } else {
        const float* p = Kc + 512 + (q - 128);
        k[0] = k[1] = k[2] = k[3] = p[0]; k[5] = k[6] = k[7] = p[1]; k[4] = (q & 1) ? p[1] : p[0];
      }
      const int se = 14 - __builtin_amdgcn_frexp_expf(Kc[641]);
      u32x4 o;
#pragma unroll
      for (int e = 0; e < 4; ++e) o[e] = pk_h(__builtin_amdgcn_ldexpf(k[7 - 2 * e], se), __builtin_amdgcn_ldexpf(k[6 - 2 * e], se));
      ((u32x4*)(ws + WS_KX))[(size_t)b * 256 + q] = o;
      if (tid == 0) ((int*)(ws + WS_MODP))[b * 24 + 3] = se;
    }
    TOUCH_DONE(pf);
  }
}

constexpr int PF_ROWS = 288;
constexpr int KCP = 4160;
constexpr int K2_H = 4 * KCP;
constexpr int K2_RED = K2_H + 4608;

extern "C" __global__ __launch_bounds__(256) void k2_kernel(const u16* __restrict__ dskip, const u16* __restrict__ w1, const u16* __restrict__ w3,
                                                            char* __restrict__ ws) {
  __shared__ __attribute__((aligned(16))) char smem[K2_RED + 24 * 1024];
  const int tid = threadIdx.x, lane = tid & 63, wid = __builtin_amdgcn_readfirstlane(tid >> 6);
  const int d = (blockIdx.x & 7) * 72 + (blockIdx.x >> 3);
  const u32x4* kx = (const u32x4*)(ws + WS_KX) + (size_t)d * 256;
  const u32x4 xh = ((const u32x4*)(ws + WS_XHT))[(size_t)tid * D + d];
  const u32x4 kA = kx[tid];
  u32x2 kB = *(const u32x2*)(kx + max(tid - 1, 0));
  const f32x4* mp = (const f32x4*)(ws + WS_MODP) + d * 6;
  float md[6];
  int se = 0;
#pragma unroll
  for (int i = 0; i < 6; ++i) {
    const f32x4 v = mp[i];
    md[i] = bfr(v[0] + v[1] + v[2]);
    if (i == 0) { const float e = v[3]; se = __builtin_bit_cast(int, e); }
  }
  md[1] = bfr(1.0f + md[1]); md[4] = bfr(1.0f + md[4]);
  const u32 dkw = ((const u32*)dskip)[d >> 1];
  const float dk = (d & 1) ? bfhi(dkw) : bflo(dkw);
  const float sa = md[0], c1 = md[1], ga = md[2];
  const int he = 14 - __builtin_amdgcn_frexp_expf(24.5f * fabsf(c1) + fabsf(sa));
  if (tid == 0) kB = u32x2{0, 0};
  {
    char* p = smem + (255 - tid) * 16;
    const u32 e0 = __builtin_amdgcn_alignbit(kA[1], kA[0], 16), e1 = __builtin_amdgcn_alignbit(kA[2], kA[1], 16);
    const u32 e2 = __builtin_amdgcn_alignbit(kA[3], kA[2], 16), e3 = __builtin_amdgcn_alignbit(kB[0], kA[3], 16);
    const u32 e4 = __builtin_amdgcn_alignbit(kB[1], kB[0], 16);
    *(u32x4*)(p + 0 * KCP) = u32x4{e1, e2, e3, e4};
    *(u32x4*)(p + 1 * KCP) = u32x4{kA[1], kA[2], kA[3], kB[0]};
    *(u32x4*)(p + 2 * KCP) = u32x4{e0, e1, e2, e3};
    *(u32x4*)(p + 3 * KCP) = kA;
    if (tid < 8) {
      *(u32x4*)(smem + (tid & 3) * KCP + (256 + (tid >> 2)) * 16) = u32x4{0, 0, 0, 0};
    }
    u32x4 hh;
#pragma unroll
    for (int i = 0; i < 4; ++i) {
      const u32 m = pk_bf(bflo(xh[i]) * c1, bfhi(xh[i]) * c1);
      const u32 h = pk_bf(bflo(m) + sa, bfhi(m) + sa);
      hh[i] = pk_h(__builtin_amdgcn_ldexpf(bflo(h), he), __builtin_amdgcn_ldexpf(bfhi(h), he));
    }
    *(u32x4*)(smem + K2_H + 512 + tid * 16) = hh;
    if (tid < 32) *(u32x4*)(smem + K2_H + tid * 16) = u32x4{0, 0, 0, 0};
  }
  u32 pf = 0;
  {
    const int i = blockIdx.x * 256 + tid;
    if (i < 2 * PF_ROWS * 72) pf = touch((const char*)(i < PF_ROWS * 72 ? w1 : w3) + (size_t)(i < PF_ROWS * 72 ? i : i - PF_ROWS * 72) * 128);
  }
  WAIT_L0(); BAR();
  if (tid == 0) {
    float* mf = (float*)(ws + WS_MODF);
#pragma unroll
    for (int i = 3; i < 6; ++i) mf[i * D + d] = md[i];
  }
  const int r = lane & 15, g = lane >> 4;
  f32x4 acc[8];
#pragma unroll
  for (int i = 0; i < 8; ++i) acc[i] = f32x4{0, 0, 0, 0};
  const char* abase = smem + (r & 3) * KCP + 2 * (2044 + 8 - 8 * g - (r & 12));
  const char* bbase = smem + K2_H + 512 + 2 * (16 * r - 8 * g + 8);
#pragma unroll
  for (int cls = 0; cls < 2; ++cls) {
    const int i0 = 2 * wid + cls;
    const char* ap = abase - 64 * (7 - i0);
    const char* bp = bbase + 64 * (i0 - 7);
    f16x8 A[8], B[8];
#pragma unroll
    for (int m = 0; m < 8; ++m) {
      const u32x2 lo = *(const volatile LAS u32x2*)(ap - 512 * m), hi = *(const volatile LAS u32x2*)(ap - 512 * m + 8);
      A[m] = __builtin_bit_cast(f16x8, u32x4{lo[0], lo[1], hi[0], hi[1]});
      B[m] = *(const f16x8*)(bp + 512 * m);
    }
#pragma unroll
    for (int n = 0; n < 8; ++n)
#pragma unroll
      for (int k = 0; k <= n; ++k) acc[n] = __builtin_amdgcn_mfma_f32_16x16x32_f16(A[n - k], B[k], acc[n], 0, 0, 0);
  }
  f32x4* red = (f32x4*)(smem + K2_RED);
#pragma unroll
  for (int n = 0; n < 8; ++n) {
    const int o = n >> 1;
    if (o != wid) red[(n * 3 + (wid > o ? wid - 1 : wid)) * 64 + lane] = acc[n];
  }
  WAIT_L0(); BAR();
#pragma unroll
  for (int e = 0; e < 2; ++e) {
    const int n = 2 * wid + e;
    f32x4 s = wid == 0 ? acc[e] : wid == 1 ? acc[2 + e] : wid == 2 ? acc[4 + e] : acc[6 + e];
#pragma unroll
    for (int j = 0; j < 3; ++j) s += red[(n * 3 + j) * 64 + lane];
    const int t = 256 * n + 16 * r + 4 * g;
    const u32x2 hv = *(const u32x2*)(smem + K2_H + 512 + 2 * t);
    u32x2 o;
#pragma unroll
    for (int j = 0; j < 2; ++j) {
      const u32 hw = hv[j];
      const f16x2 hp = __builtin_bit_cast(f16x2, hw);
      const u32 y = pk_bf(__builtin_amdgcn_ldexpf(s[2 * j], -se - he), __builtin_amdgcn_ldexpf(s[2 * j + 1], -se - he));
      const u32 sk = pk_bf(__builtin_amdgcn_ldexpf((float)hp[0], -he) * dk, __builtin_amdgcn_ldexpf((float)hp[1], -he) * dk);
      const u32 ys = pk_bf(bflo(y) + bflo(sk), bfhi(y) + bfhi(sk));
      o[j] = pk_bf(ga * bflo(ys), ga * bfhi(ys));
    }
    *(u32x2*)(ws + WS_RT + ((size_t)(t >> 3) * D + d) * 16 + (t & 4) * 2) = o;
  }
  TOUCH_DONE(pf);
}

extern "C" __global__ __launch_bounds__(576) void k3_kernel(const u16* __restrict__ x, char* __restrict__ ws) {
  __shared__ float red[2][9][8];
  const int tid = threadIdx.x, lane = tid & 63, wid = tid >> 6;
  const int tq = (blockIdx.x & 7) * 32 + (blockIdx.x >> 3);
  if (tid < 36) ((u32*)(ws + WS_FLAG))[blockIdx.x * 36 + tid] = 0;
  const int tok = lane & 7, piece = tid >> 3, ks = piece >> 2, g = piece & 3;
  const int dA = 32 * ks + 4 * g, t = 8 * tq + tok;
  const u32x2 xa = *(const u32x2*)(x + (size_t)t * D + dA), xb = *(const u32x2*)(x + (size_t)t * D + dA + 16);
  const u16* rt = (const u16*)(ws + WS_RT) + ((size_t)tq * D + dA) * 8 + tok;
  u16 rv[8];
#pragma unroll
  for (int j = 0; j < 4; ++j) { rv[j] = rt[8 * j]; rv[4 + j] = rt[128 + 8 * j]; }
  const float* mf = (const float*)(ws + WS_MODF);
  const f32x4 sbA = *(const f32x4*)(mf + 3 * D + dA), sbB = *(const f32x4*)(mf + 3 * D + dA + 16);
  const f32x4 cbA = *(const f32x4*)(mf + 4 * D + dA), cbB = *(const f32x4*)(mf + 4 * D + dA + 16);
  u32 p[4];
  p[0] = pk_bf(bflo(xa[0]) + bf2f(rv[0]), bfhi(xa[0]) + bf2f(rv[1]));
  p[1] = pk_bf(bflo(xa[1]) + bf2f(rv[2]), bfhi(xa[1]) + bf2f(rv[3]));
  p[2] = pk_bf(bflo(xb[0]) + bf2f(rv[4]), bfhi(xb[0]) + bf2f(rv[5]));
  p[3] = pk_bf(bflo(xb[1]) + bf2f(rv[6]), bfhi(xb[1]) + bf2f(rv[7]));
  float e[8];
#pragma unroll
  for (int j = 0; j < 4; ++j) { e[2 * j] = bflo(p[j]); e[2 * j + 1] = bfhi(p[j]); }
  float s = ((e[0] + e[1]) + (e[2] + e[3])) + ((e[4] + e[5]) + (e[6] + e[7]));
  s = sum_hi3(s);
  if (lane < 8) red[0][wid][lane] = s;
  __syncthreads();
  u16* x1 = (u16*)(ws + WS_X1) + (size_t)t * D + dA;
  *(u32x2*)x1 = u32x2{p[0], p[1]};
  *(u32x2*)(x1 + 16) = u32x2{p[2], p[3]};
  float mean = 0;
#pragma unroll
  for (int w = 0; w < 9; ++w) mean += red[0][w][tok];
  mean *= (1.0f / D);
  float q = 0;
#pragma unroll
  for (int j = 0; j < 8; ++j) { const float dl = e[j] - mean; q += dl * dl; }
  q = sum_hi3(q);
  if (lane < 8) red[1][wid][lane] = q;
  __syncthreads();
  float var = 0;
#pragma unroll
  for (int w = 0; w < 9; ++w) var += red[1][w][tok];
  var *= (1.0f / D);
  const float m = bfr(mean);
  const float rs = __builtin_amdgcn_rcpf(bfr(__builtin_amdgcn_sqrtf(bfr(bfr(var) + 1e-5f))));
  u32x4 o;
#pragma unroll
  for (int j = 0; j < 4; ++j) {
    const float c0 = j < 2 ? cbA[2 * j] : cbB[2 * j - 4], c1 = j < 2 ? cbA[2 * j + 1] : cbB[2 * j - 3];
    const float s0 = j < 2 ? sbA[2 * j] : sbB[2 * j - 4], s1 = j < 2 ? sbA[2 * j + 1] : sbB[2 * j - 3];
    const u32 dd = pk_bf(e[2 * j] - m, e[2 * j + 1] - m);
    const u32 xh = pk_bf(bflo(dd) * rs, bfhi(dd) * rs);
    const u32 mm = pk_bf(bflo(xh) * c0, bfhi(xh) * c1);
    o[j] = pk_bf(bflo(mm) + s0, bfhi(mm) + s1);
  }
  ((u32x4*)(ws + WS_H2T))[((size_t)(tq >> 1) * 18 + ks) * 64 + 16 * g + 8 * (tq & 1) + tok] = o;
}

__device__ __forceinline__ void glds16(const void* base, u32 voff, u32 lds) {
  asm volatile("s_mov_b32 m0, %2\n\ts_nop 0\n\tglobal_load_lds_dwordx4 %0, %1" ::"v"(voff), "s"(base), "s"(lds) : "memory");
}
__device__ __forceinline__ void glds16_q(const void* base, u32 voff, u32 lds) {
  asm volatile("s_mov_b32 m0, %2\n\ts_mov_b64 exec, 0xffff\n\tglobal_load_lds_dwordx4 %0, %1\n\ts_mov_b64 exec, -1" ::"v"(voff), "s"(base), "s"(lds) : "memory");
}
__device__ __forceinline__ void poll_ones(const u32* p, int wid, u32& a, u32& b) {
  u32 cnt = 1u << 19;
  asm volatile("s_cmp_lg_u32 %4, 1\n\ts_cbranch_scc1 2f\n\tglobal_load_dword %0, %3, off sc1\n"
               "1:\n\ts_sleep 4\n\tglobal_load_dword %1, %3, off sc1\n\ts_waitcnt vmcnt(1)\n\tv_cmp_ne_u32 vcc, 1, %0\n\ts_cbranch_vccz 2f\n\t"
               "s_sleep 4\n\tglobal_load_dword %0, %3, off sc1\n\ts_waitcnt vmcnt(1)\n\tv_cmp_ne_u32 vcc, 1, %1\n\ts_cbranch_vccz 2f\n\t"
               "s_sub_u32 %2, %2, 1\n\ts_cbranch_scc0 1b\n2:"
               : "=&v"(a), "=&v"(b), "+s"(cnt) : "v"(p), "s"(wid) : "vcc", "scc", "memory");
}
#define POLL_END(a, b) asm volatile("" ::"v"(a), "v"(b))
__device__ __forceinline__ int fresh_lane() {
  int l;
  asm volatile("v_mbcnt_lo_u32_b32 %0, -1, 0\n\tv_mbcnt_hi_u32_b32 %0, -1, %0" : "=v"(l));
  return l;
}
__device__ __forceinline__ u32 lds_addr(const void* p) { return (u32)(size_t)(const LAS char*)p; }
__device__ __forceinline__ bf16x8 tr_frag_a(u32 addr, int o2) {
  const s16x4 a = __builtin_amdgcn_ds_read_tr16_b64_v4i16((LAS s16x4*)(size_t)addr);
  const s16x4 b = __builtin_amdgcn_ds_read_tr16_b64_v4i16((LAS s16x4*)(size_t)(addr + o2));
  return __builtin_bit_cast(bf16x8, __builtin_shufflevector(a, b, 0, 1, 2, 3, 4, 5, 6, 7));
}
__device__ __forceinline__ bf16x8 tr_frag(const char* p, int o2) {
  const s16x4 a = __builtin_amdgcn_ds_read_tr16_b64_v4i16((LAS s16x4*)p);
  const s16x4 b = __builtin_amdgcn_ds_read_tr16_b64_v4i16((LAS s16x4*)(p + o2));
  return __builtin_bit_cast(bf16x8, __builtin_shufflevector(a, b, 0, 1, 2, 3, 4, 5, 6, 7));
}

__device__ __forceinline__ void st16(char* p, u32x4 v) {
  asm volatile("global_store_dwordx4 %0, %1, off sc1\n\ts_nop 1" ::"v"(p), "v"(v) : "memory");
}
constexpr int FREE = 3;
constexpr int G2_STAGE = 64 * 288, G2_LDS = 8 * G2_STAGE;

__device__ __forceinline__ void glds16_h(const void* base, u32 voff, u32 lds) {
  asm volatile("s_mov_b32 m0, %2\n\ts_mov_b32 exec_hi, 0\n\tglobal_load_lds_dwordx4 %0, %1\n\ts_mov_b32 exec_hi, -1" ::"v"(voff), "s"(base), "s"(lds) : "memory");
}
__device__ __forceinline__ constexpr int slab_row(int sg) { return 288 * (sg / 9) + (sg % 9 < 8 ? 72 * ((sg % 9) >> 1) + 32 * ((sg % 9) & 1) : 64); }

constexpr int G1_W1 = 16384, G1_W3 = G1_W1 + 9216 + 16, G1_STAGE = 35072, G1_LDS = 4 * G1_STAGE;

extern "C" __global__ __launch_bounds__(512, 2) void k4_kernel(const u16* __restrict__ w1, const u16* __restrict__ w3, const u16* __restrict__ w2,
                                                               char* __restrict__ ws, u16* __restrict__ out) {
  extern __shared__ __attribute__((aligned(16))) char smem[];
  const int tid = threadIdx.x, lane = tid & 63, wid = __builtin_amdgcn_readfirstlane(tid >> 6);
  const int mt = (blockIdx.x >> 3) & 7, ft = (blockIdx.x & 7) * 4 + (blockIdx.x >> 6);
  const int wm = wid & 3, wn = wid >> 2;
  const char* tsrc = ws + WS_H2T + (size_t)(16 * mt + 2 * wid) * 18 * 1024;
  const char* wsrc = (const char*)(wn ? w3 : w1) + (size_t)(8 * wm) * (F * 2) + ft * 288;
  const int x1 = 64 + lane, x2 = 128 + (lane & 15);
  const u32 o0 = (lane / 18) * (F * 2) + (lane % 18) * 16, o1 = (x1 / 18) * (F * 2) + (x1 % 18) * 16, o2 = (x2 / 18) * (F * 2) + (x2 % 18) * 16;
  const u32 ol = lane * 16;
  const u32 tdst = lds_addr(smem) + 2 * wid * 1024, wdst = lds_addr(smem) + (wn ? G1_W3 : G1_W1) + wm * 2304;
  auto stage = [&](int p) {
    const u32 sb = (p & 3) * G1_STAGE;
    glds16(tsrc + p * 1024, ol, sb + tdst);
    glds16(tsrc + (18 + p) * 1024, ol, sb + tdst + 1024);
    const char* wp = wsrc + (size_t)p * 32 * (F * 2);
    glds16(wp, o0, sb + wdst);
    glds16(wp, o1, sb + wdst + 1024);
    glds16_q(wp, o2, sb + wdst + 2048);
  };
  const int g = lane >> 4, q = (lane >> 2) & 3, pc = lane & 3;
  u32 boff = lds_addr(smem) + wm * 4096 + lane * 16;
  u32 aoff = lds_addr(smem) + G1_W1 + (4 * g + q) * 288 + wn * 144 + 8 * pc;
  u32 moff = lds_addr(smem) + (pc < 2 ? G1_W1 : G1_W3) + (4 * g + q) * 288 + wn * 144 + 128 + 8 * (pc & 1);
  f32x4 acc[9][4];
#pragma unroll
  for (int i = 0; i < 9; ++i)
#pragma unroll
    for (int j = 0; j < 4; ++j) acc[i][j] = f32x4{0, 0, 0, 0};

  stage(0); stage(1); stage(2);
  const char* wbase;
  u32 wvo;
  {
    constexpr u32 NL = (D - PF_ROWS) * 3 / 8;
    unsigned long long pc0;
    u32 len;
    asm volatile("s_getpc_b64 %0\n.Lk4_here:\n\ts_mov_b32 %1, kz_end-.Lk4_here" : "=s"(pc0), "=s"(len));
    const u32 i = mt * NL + min((wid & 1) * 64u + lane, NL - 1);
    wbase = wid < 4 ? (const char*)(wid < 2 ? w1 : w3) + (size_t)PF_ROWS * (F * 2) + ((ft * 288) & ~127)
                    : wid < 7 ? (const char*)w2 + (size_t)blockIdx.x * 162 * 128 : (const char*)pc0;
    wvo = wid < 4 ? (i / 3) * (F * 2) + (i % 3) * 128
                  : wid < 7 ? min((wid - 4) * 64u + lane, 161u) * 128 : min((((blockIdx.x >> 3) & 7) * 64 + lane) * 128u, len - 4);
  }
  const u32 wlds = lds_addr(smem) + G1_LDS + wid * 256;
  WAIT_V(10); BAR();
  if (wn) BAR();
#pragma unroll
  for (int p = 0; p < 18; ++p) {
    asm volatile("" : "+v"(boff), "+v"(aoff), "+v"(moff));
    bf16x8 A[9], B[4];
#pragma unroll
    for (int j = 0; j < 4; ++j) B[j] = *(const LAS bf16x8*)(size_t)(boff + j * 1024);
#pragma unroll
    for (int i = 0; i < 4; ++i) {
      A[i] = tr_frag_a(aoff + 32 * i, 16 * 288);
      A[4 + i] = tr_frag_a(aoff + (G1_W3 - G1_W1) + 32 * i, 16 * 288);
    }
    A[8] = tr_frag_a(moff, 16 * 288);
    {
      const u32 adv = (p & 3) == 3 ? -3 * G1_STAGE : G1_STAGE;
      boff += adv; aoff += adv; moff += adv;
    }
    if (p + 3 < 18) stage(p + 3);
    if (p < 8)
      asm volatile("s_mov_b32 m0, %2\n\ts_cmp_eq_u32 %3, %4\n\ts_cselect_b64 exec, -1, 0\n\tglobal_load_lds_dword %0, %1\n\ts_mov_b64 exec, -1"
                   ::"v"(wvo), "s"(wbase), "s"(wlds), "s"(wid), "i"(p) : "memory", "scc");
    constexpr int NP = 18 - FREE;
    if (p < NP) {
      if (p < 15) WAIT_V(10); else if (p == 15) WAIT_V(5); else if (p == 16) WAIT_V(0);
      WAIT_L0(); SCHED();
      BAR();
      __builtin_amdgcn_s_setprio(1);
    }
#pragma unroll
    for (int i = 0; i < 9; ++i)
#pragma unroll
      for (int j = 0; j < 4; ++j)
        asm volatile("v_mfma_f32_16x16x32_bf16 %0, %1, %2, %0" : "+v"(acc[i][j]) : "v"(A[i]), "v"(B[j]));
    if (p < NP) __builtin_amdgcn_s_setprio(0);
    SCHED();
    if (p < NP - 1) BAR();
    else if (p == NP - 1) {
      if (wn) WAIT_V(0);
      if (FREE || !wn) BAR();
      if (FREE && !wn) { WAIT_V(0); BAR(); __builtin_amdgcn_s_setprio(2); }
    }
  }
  asm volatile("s_nop 7\n\ts_nop 7" ::: "memory");
  SCHED();
  const int ln = fresh_lane(), g2 = ln >> 4, q2 = (ln >> 2) & 3, pc2 = ln & 3;
  const u32 ol2 = ln * 16;
  const int kq = blockIdx.x & 3, m2 = ((blockIdx.x >> 2) & 1) * 8 + ((blockIdx.x >> 3) & 7), cb = blockIdx.x >> 6;
  const int kg = wn, hs = wm >> 1;
  const char* gsrc = ws + WS_GT + ((size_t)(8 * m2 + 2 * wm) * 144 + 36 * kq + 18 * kg) * 1024;
  const char* w2src = (const char*)w2 + (size_t)(1152 * kq + 576 * kg) * (D * 2) + cb * 288;
  u32 wo[5];
#pragma unroll
  for (int i = 0; i < 5; ++i) { const int x = 64 * i + (i < 4 ? ln : ln & 31); wo[i] = (x / 18) * (D * 2) + (x % 18) * 16; }
  const u32 wo2l = wo[2] + (ln >= 16 ? 64 * (D * 2) : 0);
  const u32 w2dst = lds_addr(smem) + kg * 4 * G2_STAGE + wm * 4608;
  u32x4 B[4][4];
  auto issue = [&](int j, int i) {
    if (i < 5) {
      const bool left = hs ? (2 * j + 1) % 9 == 8 : (2 * j) % 9 == 8;
      const int row = (hs ? slab_row(2 * j + 1) : slab_row(2 * j)) + (left ? 144 : 16) * (wm & 1) + (left && i > 2 ? 64 : 0);
      const char* p = w2src + (size_t)row * (D * 2);
      const u32 dst = w2dst + (j & 3) * G2_STAGE + i * 1024;
      if (i == 4) glds16_h(p, wo[4], dst);
      else if (i == 2 && (j == 4 || j == 8)) glds16(p, left ? wo2l : wo[2], dst);
      else glds16(p, wo[i], dst);
    } else {
      const int f = i - 5;
      asm volatile("global_load_dwordx4 %0, %1, %2 sc1" : "=v"(B[j & 3][f]) : "v"(ol2), "s"(gsrc + ((size_t)(f & 1) * 144 + 2 * j + (f >> 1)) * 1024) : "memory");
    }
  };
  const int P = ft >> 1, qq = 2 * (ft & 1) + wn;
  char* gbase = ws + WS_GT + ((size_t)(16 * mt + 4 * wm) * 144 + 9 * P) * 1024;
  auto glu2 = [&](float g0, float g1, float u0, float u1) -> u32 {
    const f32x2 gv = {g0, g1}, uv = {u0, u1};
    const f32x2 t = gv * -1.4426950408889634f;
    const f32x2 dn = f32x2{__builtin_amdgcn_exp2f(t[0]), __builtin_amdgcn_exp2f(t[1])} + 1.0f;
    const f32x2 o = (gv * uv) * f32x2{__builtin_amdgcn_rcpf(dn[0]), __builtin_amdgcn_rcpf(dn[1])};
    return pk_bf(o[0], o[1]);
  };
#pragma unroll
  for (int j = 0; j < 4; ++j)
#pragma unroll
    for (int s = 0; s < 2; ++s) {
      u32x4 o;
#pragma unroll
      for (int h = 0; h < 2; ++h) {
        const f32x4 a = acc[2 * s + h][j], u = acc[4 + 2 * s + h][j];
        o[2 * h] = glu2(a[0], a[1], u[0], u[1]);
        o[2 * h + 1] = glu2(a[2], a[3], u[2], u[3]);
      }
      st16(gbase + (size_t)j * 144 * 1024 + (2 * qq + s) * 1024 + ln * 16, o);
    }
#pragma unroll
  for (int jp = 0; jp < 2; ++jp) {
    float a[4], u[4];
#pragma unroll
    for (int e = 0; e < 4; ++e) {
      const float m0 = acc[8][2 * jp][e], m1 = acc[8][2 * jp + 1][e];
      auto r = __builtin_amdgcn_permlane32_swap(__builtin_bit_cast(u32, m0), __builtin_bit_cast(u32, m1), false, false);
      a[e] = __builtin_bit_cast(float, (u32)r[0]); u[e] = __builtin_bit_cast(float, (u32)r[1]);
    }
    const u32x2 o = {glu2(a[0], a[1], u[0], u[1]), glu2(a[2], a[3], u[2], u[3])};
    const int j = 2 * jp + (ln >> 5);
    char* gp = gbase + (size_t)j * 144 * 1024 + 8 * 1024 + (16 * (2 * (qq & 1) + (g2 & 1)) + (ln & 15)) * 16 + 8 * (qq >> 1);
    asm volatile("global_store_dwordx2 %0, %1, off sc1\n\ts_nop 1" ::"v"(gp), "v"(o) : "memory");
  }
  u32 pa, pb;
  __builtin_amdgcn_s_setprio(0);
  WAIT_V(0); BAR();
  {
    u32* gfl = (u32*)(ws + WS_FLAG) + 8192;
    if (wid == 0) {
      if (ln == 0) __hip_atomic_store(gfl + (mt * 4 + (ft >> 3)) * 32 + (ft & 7), 1u, __ATOMIC_RELAXED, __HIP_MEMORY_SCOPE_AGENT);
    }
#pragma unroll
    for (int j = 0; j < 2; ++j)
#pragma unroll
      for (int i = 0; i < 5; ++i) issue(j, i);
    poll_ones(gfl + ((m2 >> 1) * 4 + kq) * 32 + (ln & 7), wid, pa, pb);
    BAR();
  }
#define WAIT_B(n, j) asm volatile("s_waitcnt vmcnt(" #n ")" : "+v"(B[(j) & 3][0]), "+v"(B[(j) & 3][1]), "+v"(B[(j) & 3][2]), "+v"(B[(j) & 3][3])::"memory")
  const char* abase = smem + kg * 4 * G2_STAGE + (4 * g2 + q2) * 288 + 8 * pc2;
  f32x4 ac2[9][2];
#pragma unroll
  for (int c = 0; c < 9; ++c) ac2[c][0] = ac2[c][1] = f32x4{0, 0, 0, 0};
#pragma unroll
  for (int i = 5; i < 9; ++i) issue(0, i);
  WAIT_V(9); BAR();
  if (kg) BAR();
#pragma unroll
  for (int j = 0; j < 9; ++j) {
    bf16x8 A[2][9];
#pragma unroll
    for (int h = 0; h < 2; ++h)
#pragma unroll
      for (int c = 0; c < 9; ++c) A[h][c] = tr_frag(abase + (j & 3) * G2_STAGE + h * 32 * 288 + 32 * c, 16 * 288);
#pragma unroll
    for (int i = 5; i < 9; ++i)
      if (j < 8) issue(j + 1, i);
    if (j == 0) WAIT_B(4, j); else if (j < 8) WAIT_B(9, j); else WAIT_B(0, j);
    if (j == 0) POLL_END(pa, pb);
    WAIT_L0(); SCHED();
    BAR();
    __builtin_amdgcn_s_setprio(1);
#pragma unroll
    for (int i = 0; i < 9; ++i) {
#pragma unroll
      for (int e = 0; e < 2; ++e) {
        const int h = (2 * i + e) / 9, c = (2 * i + e) % 9;
#pragma unroll
        for (int t = 0; t < 2; ++t) {
          const u32x4 bv = B[j & 3][2 * h + t];
          ac2[c][t] = __builtin_amdgcn_mfma_f32_16x16x32_bf16(A[h][c], __builtin_bit_cast(bf16x8, bv), ac2[c][t], 0, 0, 0);
        }
      }
      SCHED();
      if (j + 2 < 9 && !(i & 1)) issue(j + 2, i >> 1);
      SCHED();
    }
    __builtin_amdgcn_s_setprio(0);
    if (j < 7) WAIT_V(9); else if (j == 7) WAIT_V(4);
    SCHED();
    if (j < 8 || !kg) BAR();
  }
  const int et = kg, ep = wm;
  const size_t orow = (size_t)(128 * m2 + 32 * kq + 16 * et + (ln & 15)) * D + 144 * cb + 4 * g2;
  f32x4 gb[3]; u32x2 xr[3];
#pragma unroll
  for (int c = 0; c < 3; ++c) {
    const int ct = c < 2 ? 2 * ep + c : 8;
    gb[c] = *(const f32x4*)((const float*)(ws + WS_MODF) + 5 * D + 144 * cb + 16 * ct + 4 * g2);
    xr[c] = *(const u32x2*)((const u16*)(ws + WS_X1) + orow + 16 * ct);
  }
  f32x4* red = (f32x4*)smem;
#pragma unroll
  for (int c = 0; c < 9; ++c) red[(wid * 9 + c) * 64 + ln] = kg ? ac2[c][0] : ac2[c][1];
  WAIT_L0(); BAR();
  const int tile = m2 * 4 + cb;
  char* xch = ws + WS_XCH + (size_t)tile * (12 * 9216);
  f32x4* own = red + 72 * 64;
  {
    f32x4 v[9];
#pragma unroll
    for (int c = 0; c < 9; ++c) v[c] = (kg ? ac2[c][1] : ac2[c][0]) + red[((wid ^ 4) * 9 + c) * 64 + ln];
    if (wm == kq) {
#pragma unroll
      for (int c = 0; c < 9; ++c) own[(kg * 9 + c) * 64 + ln] = v[c];
    } else {
      char* p = xch + ((wm * 3 + (kq > wm ? kq - 1 : kq)) * 2 + kg) * 4608;
#pragma unroll
      for (int c = 0; c < 4; ++c) {
        const u32x4 w = {pk_bf(v[2 * c][0], v[2 * c][1]), pk_bf(v[2 * c][2], v[2 * c][3]), pk_bf(v[2 * c + 1][0], v[2 * c + 1][1]),
                         pk_bf(v[2 * c + 1][2], v[2 * c + 1][3])};
        asm volatile("global_store_dwordx4 %0, %1, off sc1\n\ts_nop 1" ::"v"(p + c * 1024 + ln * 16), "v"(w) : "memory");
      }
      const u32x2 w = {pk_bf(v[8][0], v[8][1]), pk_bf(v[8][2], v[8][3])};
      asm volatile("global_store_dwordx2 %0, %1, off sc1\n\ts_nop 1" ::"v"(p + 4096 + ln * 8), "v"(w) : "memory");
    }
  }
  asm volatile("s_waitcnt vmcnt(0) lgkmcnt(0)" : "+v"(gb[0]), "+v"(gb[1]), "+v"(gb[2]), "+v"(xr[0]), "+v"(xr[1]), "+v"(xr[2])::"memory");
  BAR();
  u32* flags = (u32*)(ws + WS_FLAG) + tile * 128;
  if (wid == 0) {
    if (ln == 0) {
#pragma unroll
      for (int r = 0; r < 4; ++r)
        if (r != kq) __hip_atomic_store(flags + r * 32 + (kq > r ? kq - 1 : kq), 1u, __ATOMIC_RELAXED, __HIP_MEMORY_SCOPE_AGENT);
    }
  }
  poll_ones(flags + kq * 32 + min(ln, 2), wid, pa, pb);
  BAR();
  {
    u32x4 pr[3]; u32x2 pl[3];
    const char* src = xch + ((size_t)kq * 6 + et) * 4608;
    const char *s4 = src + ep * 1024 + ln * 16, *s2 = src + 4096 + ln * 8;
    asm volatile("global_load_dwordx4 %0, %6, off sc1\n\tglobal_load_dwordx4 %1, %7, off sc1\n\tglobal_load_dwordx4 %2, %8, off sc1\n\t"
                 "global_load_dwordx2 %3, %9, off sc1\n\tglobal_load_dwordx2 %4, %10, off sc1\n\tglobal_load_dwordx2 %5, %11, off sc1\n\t"
                 "s_waitcnt vmcnt(0)"
                 : "=&v"(pr[0]), "=&v"(pr[1]), "=&v"(pr[2]), "=&v"(pl[0]), "=&v"(pl[1]), "=&v"(pl[2])
                 : "v"(s4), "v"(s4 + 9216), "v"(s4 + 18432), "v"(s2), "v"(s2 + 9216), "v"(s2 + 18432) : "memory");
    POLL_END(pa, pb);
#pragma unroll
    for (int c = 0; c < 3; ++c) {
      const int ct = c < 2 ? 2 * ep + c : 8;
      f32x4 s = own[(et * 9 + ct) * 64 + ln];
#pragma unroll
      for (int k = 0; k < 3; ++k) {
        const u32 lo = c < 2 ? pr[k][2 * c] : pl[k][0], hi = c < 2 ? pr[k][2 * c + 1] : pl[k][1];
        s += f32x4{bflo(lo), bfhi(lo), bflo(hi), bfhi(hi)};
      }
      u32x2 o;
#pragma unroll
      for (int h = 0; h < 2; ++h) {
        const u32 y = pk_bf(s[2 * h], s[2 * h + 1]);
        const u32 z = pk_bf(gb[c][2 * h] * bflo(y), gb[c][2 * h + 1] * bfhi(y));
        o[h] = pk_bf(bflo(xr[c][h]) + bflo(z), bfhi(xr[c][h]) + bfhi(z));
      }
      if (c < 2 || ep == 0) *(u32x2*)(out + orow + 16 * ct) = o;
    }
  }
}


extern "C" __device__ __attribute__((used, noinline)) void kz_end() {}

extern "C" void kernel_launch(void* const* d_in, const int* in_sizes, int n_in, void* d_out, int out_size, void* d_ws, size_t ws_size,
                              hipStream_t stream) {
  const u16* u = (const u16*)d_in[0];
  char* ws = (char*)d_ws;
  (void)hipFuncSetAttribute((const void*)k4_kernel, hipFuncAttributeMaxDynamicSharedMemorySize, G2_LDS);
  k1_kernel<<<K1_MOD + K1_LN + K1_KG, 256, 0, stream>>>(u, (const u16*)d_in[1], (const u16*)d_in[2], (const u16*)d_in[3], (const u16*)d_in[4],
                                                        (const u16*)d_in[5], (const u16*)d_in[6], (const u16*)d_in[7], (const u16*)d_in[13],
                                                        (const u16*)d_in[14], ws);
  k2_kernel<<<D, 256, 0, stream>>>((const u16*)d_in[8], (const u16*)d_in[10], (const u16*)d_in[11], ws);
  k3_kernel<<<256, 576, 0, stream>>>(u, ws);
  k4_kernel<<<256, 512, G2_LDS, stream>>>((const u16*)d_in[10], (const u16*)d_in[11], (const u16*)d_in[12], ws, (u16*)d_out);
}
```
